```python
import torch

def _softmax(x, axis=-1):
    return torch.softmax(x, dim=axis)
def _gelu(x):
    return torch.nn.functional.gelu(x, approximate="tanh")
def _silu(x):
    return torch.nn.functional.silu(x)
def _sigmoid(x):
    return torch.sigmoid(x)
def _layernorm(x, axis=-1, eps=1e-5):
    m = x.mean(dim=axis, keepdim=True)
    v = x.var(dim=axis, keepdim=True, unbiased=False)
    return (x - m) / torch.sqrt(v + eps)
def _scatter_add(tgt, idx, src, axis=0):
    return tgt.clone().index_add_(axis, idx, src)
def _segment_sum(src, seg, n):
    out = torch.zeros((n,) + src.shape[1:], dtype=src.dtype)
    return out.index_add_(0, seg, src)
def _pad(x, pw):
    flat = tuple(v for pair in reversed(pw) for v in pair)
    return torch.nn.functional.pad(x, flat)

kernel_name = 'standalone__mixer_s_mlp__sampling'

def setup_inputs():

    _w = (lambda a: (lambda t: t.to(torch.bfloat16) if t.is_floating_point() else t)(torch.as_tensor(a)))
    B, S, D, F = 32, 32, 576, 4608
    return {
        "x":  _w((lambda *s: torch.randn(*s, dtype=torch.bfloat16) * 0.4)(B, S, D)),
        "w1": _w((lambda *s: torch.randn(*s, dtype=torch.bfloat16) * 3.2)(D, D) * (D ** -0.5)),
        "w2": _w((lambda *s: torch.randn(*s, dtype=torch.bfloat16) * 3.2)(D, D) * (D ** -0.5)),
        "w3": _w((lambda *s: torch.randn(*s, dtype=torch.bfloat16) * 3.2)(D, F) * (D ** -0.5)),
        "w4": _w((lambda *s: torch.randn(*s, dtype=torch.bfloat16) * 0.8)(F, D) * (F ** -0.5)),
    }

def reference(x, w1, w2, w3, w4):
    B, S, D = x.shape
    h = _gelu(_layernorm(x, axis=-1) @ w1)
    c = D // 4
    g0 = _pad(h[:, :, 0*c:1*c], ((0, 0), (1, 0), (0, 0)))[:, :S, :]
    g1 = _pad(h[:, :, 1*c:2*c], ((0, 0), (0, 1), (0, 0)))[:, 1:, :]
    g2 = _pad(h[:, :, 2*c:3*c], ((0, 0), (2, 0), (0, 0)))[:, :S, :]
    g3 = _pad(h[:, :, 3*c:   ], ((0, 0), (0, 2), (0, 0)))[:, 2:, :]
    hs = torch.concatenate([g0, g1, g2, g3], axis=-1) @ w2
    x1 = x + hs
    h2 = _gelu(_layernorm(x1, axis=-1) @ w3) @ w4
    return x1 + h2
```

```cpp
#include <hip/hip_runtime.h>
#include <hip/hip_bf16.h>
#include <stdint.h>

typedef short bf16x8 __attribute__((ext_vector_type(8)));
typedef float f32x4 __attribute__((ext_vector_type(4)));
typedef unsigned short u16;

constexpr int Bn = 32, Sn = 32, D = 576, F = 4608, M = Bn * Sn;

__device__ __forceinline__ float bf2f(u16 v) { return __uint_as_float(((unsigned)v) << 16); }
__device__ __forceinline__ u16 f2bf(float f) { __hip_bfloat16 b = __float2bfloat16(f); return *(u16*)&b; }
__device__ __forceinline__ float gelu(float x) {
    float u = 0.7978845608028654f * (x + 0.044715f * x * x * x);
    float t = 1.0f - 2.0f / (1.0f + __expf(2.0f * u));
    return 0.5f * x * (1.0f + t);
}

__global__ void k_ln(const u16* __restrict__ in, u16* __restrict__ out) {
    int row = blockIdx.x; int lane = threadIdx.x;
    float v[9]; float s = 0.f;
    for (int i = 0; i < 9; ++i) { v[i] = bf2f(in[row * D + lane + 64 * i]); s += v[i]; }
    for (int o = 32; o > 0; o >>= 1) s += __shfl_xor(s, o);
    float mean = s / D; float q = 0.f;
    for (int i = 0; i < 9; ++i) { float d = v[i] - mean; q += d * d; }
    for (int o = 32; o > 0; o >>= 1) q += __shfl_xor(q, o);
    float r = rsqrtf(q / D + 1e-5f);
    for (int i = 0; i < 9; ++i) out[row * D + lane + 64 * i] = f2bf((v[i] - mean) * r);
}

template <int MODE>
__global__ void k_gemm(const u16* __restrict__ A, const u16* __restrict__ W, const u16* __restrict__ res,
                       u16* __restrict__ C, int N, int K) {
    int lane = threadIdx.x; int r = lane & 15, q = lane >> 4;
    int m0 = blockIdx.y * 16, n0 = blockIdx.x * 16;
    f32x4 acc = {0, 0, 0, 0};
    for (int k0 = 0; k0 < K; k0 += 32) {
        bf16x8 a, b;
        int k = k0 + q * 8;
        int row = m0 + r;
        if (MODE == 1) {
            int g = k / 144; int sh = (g == 0) ? -1 : (g == 1) ? 1 : (g == 2) ? -2 : 2;
            int s = (row & 31) + sh;
            if (s >= 0 && s < 32) a = *(const bf16x8*)(A + (size_t)(row + sh) * K + k);
            else a = bf16x8{0, 0, 0, 0, 0, 0, 0, 0};
        } else a = *(const bf16x8*)(A + (size_t)row * K + k);
        for (int j = 0; j < 8; ++j) b[j] = (short)W[(size_t)(k + j) * N + n0 + r];
        acc = __builtin_amdgcn_mfma_f32_16x16x32_bf16(a, b, acc, 0, 0, 0);
    }
    for (int j = 0; j < 4; ++j) {
        int row = m0 + q * 4 + j, col = n0 + r;
        float v = acc[j];
        if (MODE == 0) v = gelu(v);
        else v = bf2f(f2bf(v)) + bf2f(res[(size_t)row * N + col]);
        C[(size_t)row * N + col] = f2bf(v);
    }
}

extern "C" void kernel_launch(void* const* d_in, const int* in_sizes, int n_in,
                              void* d_out, int out_size, void* d_ws, size_t ws_size,
                              hipStream_t stream) {
    const u16* x = (const u16*)d_in[0];
    const u16* w1 = (const u16*)d_in[1];
    const u16* w2 = (const u16*)d_in[2];
    const u16* w3 = (const u16*)d_in[3];
    const u16* w4 = (const u16*)d_in[4];
    u16* out = (u16*)d_out;
    u16* ws = (u16*)d_ws;
    u16* xn = ws;
    u16* h = xn + M * D;
    u16* x1 = h + M * D;
    u16* x1n = x1 + M * D;
    u16* H = x1n + M * D;
    k_ln<<<M, 64, 0, stream>>>(x, xn);
    k_gemm<0><<<dim3(D / 16, M / 16), 64, 0, stream>>>(xn, w1, nullptr, h, D, D);
    k_gemm<1><<<dim3(D / 16, M / 16), 64, 0, stream>>>(h, w2, x, x1, D, D);
    k_ln<<<M, 64, 0, stream>>>(x1, x1n);
    k_gemm<0><<<dim3(F / 16, M / 16), 64, 0, stream>>>(x1n, w3, nullptr, H, F, D);
    k_gemm<2><<<dim3(D / 16, M / 16), 64, 0, stream>>>(H, w4, x1, out, D, F);
}
```

```cpp
#include <hip/hip_runtime.h>
#include <stdint.h>

typedef unsigned short u16;
typedef unsigned v4u __attribute__((ext_vector_type(4)));
typedef unsigned v2u __attribute__((ext_vector_type(2)));
typedef short s16x4 __attribute__((ext_vector_type(4)));
typedef short bf16x8 __attribute__((ext_vector_type(8)));
typedef float f32x4 __attribute__((ext_vector_type(4)));
typedef float f32x2 __attribute__((ext_vector_type(2)));
typedef __bf16 bf16x2 __attribute__((ext_vector_type(2)));
#define LAS __attribute__((address_space(3)))
#define DI __device__ __forceinline__

#define WS_FLAG 0
#define WS_HF 262144
#define WS_X1F (WS_HF + 1179648)
#define WS_ST (WS_X1F + 1179648)
#define WS_HH (WS_ST + 73728)
#define WS_PP (WS_HH + 9437184)
#define WS_DBG (WS_PP + 9437184)
#define WS_TOTAL (WS_DBG + 131072)
constexpr int FT = 0x8000, F1_OFF = 0x100, F2_OFF = 0x800, F3_OFF = 0x2800, F4_OFF = 0x3800;
constexpr int LA = 0, LB = 73728, LC = 147456, LM = 161792;
constexpr int SPIN_CAP = 40000;
#define WARM_SLEEP 24

DI v4u mk_srd(const void* p, unsigned bytes) {
    unsigned long long a = (unsigned long long)p;
    v4u s; s.x = (unsigned)a; s.y = (unsigned)(a >> 32) & 0xffffu; s.z = bytes; s.w = 0x20000u; return s;
}
DI void glds(unsigned voff, v4u srd, unsigned soff, unsigned lds) {
    asm volatile("s_mov_b32 m0, %3\n\ts_nop 0\n\tbuffer_load_dwordx4 %0, %1, %2 offen lds" ::"v"(voff), "s"(srd), "s"(soff), "s"(lds));
}
#define LD4A(d, voff, srd, soff, imm) asm volatile("buffer_load_dwordx4 %0, %1, %2, %3 offen offset:%4 sc1" : "=a"(d) : "v"(voff), "s"(srd), "s"(soff), "i"(imm))
#define LD4V(d, voff, srd, soff, imm) asm volatile("buffer_load_dwordx4 %0, %1, %2, %3 offen offset:%4 sc1" : "=v"(d) : "v"(voff), "s"(srd), "s"(soff), "i"(imm))
#define LD2V(d, voff, srd, soff, imm) asm volatile("buffer_load_dwordx2 %0, %1, %2, %3 offen offset:%4 sc1" : "=v"(d) : "v"(voff), "s"(srd), "s"(soff), "i"(imm))
#define LD2A(d, voff, srd, soff, imm) asm volatile("buffer_load_dwordx2 %0, %1, %2, %3 offen offset:%4 sc1" : "=a"(d) : "v"(voff), "s"(srd), "s"(soff), "i"(imm))
#define LD4W(d, voff, srd, soff) asm volatile("buffer_load_dwordx4 %0, %1, %2, %3 offen" : "+v"(d) : "v"(voff), "s"(srd), "s"(soff))
#define ST4(d, voff, srd, soff, imm) asm volatile("buffer_store_dwordx4 %0, %1, %2, %3 offen offset:%4 sc1\n\ts_nop 1" ::"v"(d), "v"(voff), "s"(srd), "s"(soff), "i"(imm))
#define ST2(d, voff, srd, soff, imm) asm volatile("buffer_store_dwordx2 %0, %1, %2, %3 offen offset:%4 sc1" ::"v"(d), "v"(voff), "s"(srd), "s"(soff), "i"(imm))
#define ST2P(d, voff, srd, soff, imm) asm volatile("buffer_store_dwordx2 %0, %1, %2, %3 offen offset:%4" ::"v"(d), "v"(voff), "s"(srd), "s"(soff), "i"(imm))
#define WAITV(n) asm volatile("s_waitcnt vmcnt(%0)" ::"i"(n) : "memory")
#define WAITV_A2(n, a, b) asm volatile("s_waitcnt vmcnt(%2)" : "+a"(a), "+a"(b) : "i"(n))
#define WAITV_V2(n, a, b) asm volatile("s_waitcnt vmcnt(%2)" : "+v"(a), "+v"(b) : "i"(n))
#define WAITV_V1(n, a) asm volatile("s_waitcnt vmcnt(%1)" : "+v"(a) : "i"(n))
#define BARV(n) asm volatile("s_waitcnt vmcnt(%0) lgkmcnt(0)\n\ts_barrier" ::"i"(n) : "memory")
#define BARL() asm volatile("s_waitcnt lgkmcnt(0)\n\ts_barrier" ::: "memory")
#define SB() __builtin_amdgcn_sched_barrier(0)
#define MFMA_VA(c, a, b) asm volatile("v_mfma_f32_16x16x32_bf16 %0, %1, %2, %0" : "+v"(c) : "v"(a), "a"(b))
#define MFMA_VV(c, a, b) asm volatile("v_mfma_f32_16x16x32_bf16 %0, %1, %2, %0" : "+v"(c) : "v"(a), "v"(b))
#define MFMA_VV0(c, a, b) asm volatile("v_mfma_f32_16x16x32_bf16 %0, %1, %2, 0" : "=&v"(c) : "v"(a), "v"(b))
#define MFMA_VA0(c, a, b) asm volatile("v_mfma_f32_16x16x32_bf16 %0, %1, %2, 0" : "=&v"(c) : "v"(a), "a"(b))
#define GL_INC(voff, srd, so, lw, limm, sinc) asm volatile("s_add_u32 m0, %3, %4\n\ts_add_u32 %0, %0, %5\n\tbuffer_load_dwordx4 %1, %2, %0 offen lds" : "+s"(so) : "v"(voff), "s"(srd), "s"(lw), "i"(limm), "i"(sinc) : "scc")
#define GL_Q(voff, srd, so, lw, limm, off) asm volatile("s_add_u32 m0, %3, %4\n\ts_nop 0\n\tbuffer_load_dwordx4 %0, %1, %2 offen offset:%5 lds" ::"v"(voff), "s"(srd), "s"(so), "s"(lw), "i"((limm) - (off)), "i"(off) : "scc")
#define OPQ(x) asm volatile("" : "+v"(x))
#define NOP8() asm volatile("s_nop 7\n\ts_nop 1" ::: "memory")

DI v2u trd(unsigned addr) { return __builtin_bit_cast(v2u, __builtin_amdgcn_ds_read_tr16_b64_v4i16((LAS s16x4*)addr)); }
DI v4u cat(v2u a, v2u b) { v4u r; r.x = a.x; r.y = a.y; r.z = b.x; r.w = b.y; return r; }
DI f32x4 mfma_b(v4u a, v4u b, f32x4 c) {
    return __builtin_amdgcn_mfma_f32_16x16x32_bf16(__builtin_bit_cast(bf16x8, a), __builtin_bit_cast(bf16x8, b), c, 0, 0, 0);
}
DI unsigned pk2(float a, float b) { bf16x2 p; p.x = (__bf16)a; p.y = (__bf16)b; return __builtin_bit_cast(unsigned, p); }
DI float blo(unsigned u) { return __uint_as_float(u << 16); }
DI float bhi(unsigned u) { return __uint_as_float(u & 0xffff0000u); }
DI float xsum4(float x) {
    auto s = __builtin_amdgcn_permlane16_swap(__float_as_uint(x), __float_as_uint(x), false, false);
    x = __uint_as_float(s[0]) + __uint_as_float(s[1]);
    auto t = __builtin_amdgcn_permlane32_swap(__float_as_uint(x), __float_as_uint(x), false, false);
    return __uint_as_float(t[0]) + __uint_as_float(t[1]);
}
constexpr float GK1 = -2.3022081986f, GK2 = -0.1029432397f;
DI float gelu1(float z) {
    float e = __builtin_amdgcn_exp2f(z * __builtin_fmaf(z * z, GK2, GK1));
    return z * __builtin_amdgcn_rcpf(1.0f + e);
}
DI void poll(unsigned voff, v4u srd, unsigned soff, v2u tag, int& dead) {
    if (dead) return;
    for (int it = 0;; ++it) {
        v2u f;
        asm volatile("buffer_load_dwordx2 %0, %1, %2, %3 offen sc1\n\ts_waitcnt vmcnt(0)" : "=&v"(f) : "v"(voff), "s"(srd), "s"(soff) : "memory");
        if (__all(f.x == tag.x && f.y == tag.y)) return;
        if (it > SPIN_CAP) { dead = 1; return; }
        __builtin_amdgcn_s_sleep(1);
    }
}
#define POLL_CHECK(n, f, voff, srd, soff, tag, dead) \
    do { WAITV_V1(n, f); if (!__all(f.x == tag.x && f.y == tag.y)) poll(voff, srd, soff, tag, dead); } while (0)

#define TS(i) do {} while (0)

DI unsigned gelu_pair(f32x2 v, float c1, float c2, float ia) {
    const f32x2 t = v * __builtin_elementwise_fma(v * v, f32x2{c2, c2}, f32x2{c1, c1});
    f32x2 e; e.x = __builtin_amdgcn_exp2f(t.x); e.y = __builtin_amdgcn_exp2f(t.y);
    const f32x2 d = __builtin_elementwise_fma(e, f32x2{ia, ia}, f32x2{ia, ia});
    f32x2 r; r.x = __builtin_amdgcn_rcpf(d.x); r.y = __builtin_amdgcn_rcpf(d.y);
    const f32x2 y = v * r;
    return pk2(y.x, y.y);
}
DI v2u gelu_acc(f32x4 a, f32x4 cs, float nm, float c1, float c2, float ia) {
    const f32x4 v = __builtin_elementwise_fma(cs, f32x4{nm, nm, nm, nm}, a);
    v2u o; o.x = gelu_pair(f32x2{v[0], v[1]}, c1, c2, ia); o.y = gelu_pair(f32x2{v[2], v[3]}, c1, c2, ia);
    return o;
}

__global__ __launch_bounds__(256, 1) void mixer_kernel(const u16* __restrict__ x, const u16* __restrict__ w1, const u16* __restrict__ w2,
                                                       const u16* __restrict__ w3, const u16* __restrict__ w4, u16* __restrict__ out,
                                                       unsigned char* __restrict__ ws) {
    __shared__ __attribute__((aligned(1024))) unsigned char smem[163840];
    TS(0);
    const int b = blockIdx.x, xcd = b & 7, j = b >> 3, team = j & 7, sub = j >> 3;
    const int lane = threadIdx.x & 63;
    int w = __builtin_amdgcn_readfirstlane(threadIdx.x >> 6);
    const int m = lane & 15, g = lane >> 4, q = m >> 2, r8 = lane >> 3, pos = lane & 7;
    const unsigned L0 = (unsigned)(size_t)(LAS unsigned char*)smem, lw = L0 + w * 1024;
    const v4u sX = mk_srd(x, 1179648), sW1 = mk_srd(w1, 663552), sW2 = mk_srd(w2, 663552), sW3 = mk_srd(w3, 5308416),
              sW4 = mk_srd(w4, 5308416), sO = mk_srd(out, 1179648), sWS = mk_srd(ws, WS_TOTAL);
    const unsigned vl16 = lane * 16, vl8 = lane * 8;
    constexpr unsigned OOB = 0x80000000u;
    const unsigned vone = lane == 0 ? 0u : OOB;
    const unsigned swzc = (((pos >> 1) ^ ((r8 >> 1) & 3)) << 5) + (pos & 1) * 16;
    const unsigned vwD = r8 * 1152 + swzc, vwF = r8 * 9216 + swzc;
    const unsigned vpD = (lane >> 1) * 1152 + (lane & 1) * 16, vpF = (lane >> 1) * 9216 + (lane & 1) * 16;
    const unsigned n3 = (576 * xcd + 128 * sub) * 2, n3c = (576 * xcd + 512 + 16 * sub) * 2;
    const bool act = j < 18;
    int dead = 0;
    v2u tagR, tagT;
    unsigned lap[8];
    {
        const int r4 = 4 * (g & 1) + q, xp = (r4 >> 1) & 3;
#pragma unroll
        for (int t = 0; t < 4; ++t) {
            lap[t] = L0 + LA + (g >> 1) * 1024 + r4 * 128 + ((t ^ xp) << 5) + (lane & 3) * 8;
            lap[4 + t] = lap[t] + (LB - LA);
            OPQ(lap[4 + t]);
        }
    }
    v4u ones = {0x3f803f80u, 0x3f803f80u, 0x3f803f80u, 0x3f803f80u};
    OPQ(ones);
#define LOAD_EPOCHS() do { LD2V(tagR, 0u, sWS, xcd * FT, 0); LD2V(tagT, 0u, sWS, team * FT, 0); } while (0)
#define TAKE_EPOCHS(n) do { WAITV_V2(n, tagR, tagT); tagR.x += 1; tagT.x += 1; } while (0)
#define HEAD_PIECE(i) ((w + 4 * (i) < 14) ? w + 4 * (i) : w + 8)

    if (act) {
        const int rh = (j * 29) >> 8, c9 = j - 9 * rh, tok0 = xcd * 128 + rh * 64;
        const unsigned vx0 = r8 * 1152 + ((pos ^ (r8 >> 1)) << 4), vx1 = r8 * 1152 + ((pos ^ ((r8 >> 1) | 4)) << 4);
        const unsigned vw1 = r8 * 1152 + (((pos >> 1) ^ (((r8 >> 1) & 1) | ((w & 1) << 1))) << 5) + (pos & 1) * 16;
#define G1_LINE(u) (c9 + (u) >= 9 ? c9 + (u)-9 : c9 + (u))
#define G1_X(u) do { glds(vx0, sX, (tok0 + 16 * w) * 1152 + G1_LINE(u) * 128, L0 + LB + (2 * w * 9 + (u)) * 1024); \
                     glds(vx1, sX, (tok0 + 16 * w + 8) * 1152 + G1_LINE(u) * 128, L0 + LB + ((2 * w + 1) * 9 + (u)) * 1024); } while (0)
#define G1_W(u) do { glds(vw1, sW1, (G1_LINE(u) * 64 + 8 * w) * 1152 + c9 * 128, lw + LA + (u)*8192); \
                     glds(vw1, sW1, (G1_LINE(u) * 64 + 8 * w + 32) * 1152 + c9 * 128, lw + LA + (u)*8192 + 4096); } while (0)
        LOAD_EPOCHS();
#pragma unroll
        for (int u = 0; u < 6; ++u) { G1_X(u); G1_W(u); }
        TAKE_EPOCHS(24);
        const int swz = (m >> 1) & 7;
        const unsigned xrow = L0 + LB + (2 * w + (m >> 3)) * 9216 + (m & 7) * 128;
        unsigned xa0 = xrow + ((g ^ swz) << 4), xa1 = xrow + ((g ^ swz ^ 4) << 4);
        OPQ(xa0); OPQ(xa1);
        unsigned la1[4];
        {
            const int x1 = (q >> 1) | ((g & 1) << 1);
#pragma unroll
            for (int t = 0; t < 4; ++t) la1[t] = L0 + LA + g * 1024 + q * 128 + ((t ^ w ^ x1) << 5) + (lane & 3) * 8;
        }
        f32x4 acc[4], acs, asum;
        float ssq = 0.f;
        v2u xres[4], wl[2][4], wh[2][4];
        v4u xf[2];
#define G1_RDX(s) xf[(s)&1] = *(LAS v4u*)(((s)&1 ? xa1 : xa0) + ((s) >> 1) * 1024)
#define G1_RDL(s, t) wl[(s)&1][t] = trd(la1[t] + (s)*4096)
#define G1_RDH(s, t) wh[(s)&1][t] = trd(la1[t] + (s)*4096 + 512)
#pragma unroll
        for (int s = 0; s < 18; ++s) {
            if (s == 0) { BARV(12); TS(1); }
            if (s == 6) BARV(12);
            if (s == 12) BARV(4);
            if (s == 0 || s == 6 || s == 12) {
                G1_RDX(s);
#pragma unroll
                for (int t = 0; t < 4; ++t) { G1_RDL(s, t); G1_RDH(s, t); }
            }
            if (s == 0) {
#pragma unroll
                for (int t = 0; t < 4; ++t) xres[t] = *(LAS v2u*)(xrow + (((2 * t + (g >> 1)) ^ swz) << 4) + (g & 1) * 8);
            }
            if (s < 6) { if (s & 1) G1_W(6 + (s >> 1)); else G1_X(6 + (s >> 1)); }
            else if (s < 10) glds(vpF, sW3, HEAD_PIECE(s - 6) * 32 * 9216 + n3c, L0 + LC + HEAD_PIECE(s - 6) * 1024);
            const bool nx = s != 5 && s != 11 && s != 17;
            const v4u xc = xf[s & 1];
            if (nx) G1_RDX(s + 1);
#pragma unroll
            for (int t = 0; t < 4; ++t) {
                const v4u wf = cat(wl[s & 1][t], wh[s & 1][t]);
                if (s == 0) MFMA_VV0(acc[t], wf, xc); else MFMA_VV(acc[t], wf, xc);
                if (nx) G1_RDL(s + 1, t);
                if (t == 0) { if (s == 0) MFMA_VV0(acs, wf, ones); else MFMA_VV(acs, wf, ones); }
                if (t == 1) { if (s == 0) MFMA_VV0(asum, ones, xc); else MFMA_VV(asum, ones, xc); }
                if (nx) G1_RDH(s + 1, t);
                const unsigned xe = xc[t];
                ssq = __builtin_amdgcn_fdot2_f32_bf16(__builtin_bit_cast(bf16x2, xe), __builtin_bit_cast(bf16x2, xe), ssq, false);
                SB();
            }
        }
        NOP8();
        asm volatile("" : "+v"(acs), "+v"(asum), "+v"(acc[0]), "+v"(acc[1]), "+v"(acc[2]), "+v"(acc[3]));
        if (m == 0) *(LAS f32x4*)(L0 + LM + w * 64 + g * 16) = acs;
        BARL();
        TS(2);
        const unsigned fb = xcd * FT + (rh * 2 + (w >> 1)) * 256, fi = (c9 * 2 + (w & 1)) * 8;
        {
            const float mean = asum[0] * (1.0f / 576.0f);
            const float var = __builtin_fmaxf(__builtin_fmaf(-mean, mean, xsum4(ssq) * (1.0f / 576.0f)), 0.f) + 1e-5f;
            const float a = __builtin_amdgcn_rsqf(var);
            const int tok = 16 * (w & 1) + m, batch = xcd * 4 + rh * 2 + (w >> 1);
            v2u o[4];
#pragma unroll
            for (int t = 0; t < 4; ++t) o[t] = gelu_acc(acc[t], *(LAS f32x4*)(L0 + LM + (t ^ w) * 64 + g * 16), -mean, a * GK1, a * a * a * GK2, var * a);
#pragma unroll
            for (int pp = 0; pp < 2; ++pp) {
                const v2u lo = (w & 1) ? o[2 * pp + 1] : o[2 * pp], hi = (w & 1) ? o[2 * pp] : o[2 * pp + 1];
                const int Te = 4 * c9 + 2 * ((w >> 1) ^ pp);
                const unsigned so = WS_HF + (batch * 36 + (Te >> 1)) * 1024;
                const int sh = Te < 9 ? 1 : Te < 18 ? -1 : Te < 27 ? 2 : -2, dest = tok + sh;
                const bool in = dest >= 0 && dest < 32;
                if (Te == 8 || Te == 26) {
                    const int d2 = tok - sh;
                    const bool in2 = d2 >= 0 && d2 < 32;
                    ST2((in ? lo : v2u{0u, 0u}), ((dest >> 4) & 1) * 18432 + g * 128 + (dest & 15) * 8, sWS, so, 0);
                    ST2((in2 ? hi : v2u{0u, 0u}), ((d2 >> 4) & 1) * 18432 + g * 128 + (d2 & 15) * 8, sWS, so, 512);
                } else {
                    ST4((in ? cat(lo, hi) : v4u{0u, 0u, 0u, 0u}), ((dest >> 4) & 1) * 18432 + g * 256 + (dest & 15) * 16, sWS, so, 0);
                }
            }
            TS(16);
            WAITV(0);
            ST2(tagR, vone, sWS, fb + F1_OFF + fi, 0);
        }
        TS(3);
        {
            unsigned so = (8 * w - 32) * 1152 + c9 * 128;
#pragma unroll
            for (int i = 0; i < 18; ++i) GL_INC(vwD, sW2, so, lw, LB + i * 4096, 32 * 1152);
        }
        poll((lane < 18 ? lane : 0) * 8, sWS, fb + F1_OFF, tagR, dead);
        TS(4);
        const int tile = xcd * 8 + rh * 4 + w;
        v4u hf[18];
        v2u hs[4];
#pragma unroll
        for (int s = 0; s < 18; ++s) {
            const unsigned so = WS_HF + (tile * 18 + (s & ~3)) * 1024;
            if (s == 4 || s == 13) { LD2V(hs[s == 4 ? 0 : 2], vl8, sWS, so, (s & 3) * 1024); LD2V(hs[s == 4 ? 1 : 3], vl8, sWS, so, (s & 3) * 1024 + 512); }
            else LD4V(hf[s], vl16, sWS, so, (s & 3) * 1024);
        }
        BARL();
#pragma unroll
        for (int t = 0; t < 4; ++t) { wl[0][t] = trd(lap[4 + t]); wh[0][t] = trd(lap[4 + t] + 2048); }
        unsigned so3 = (8 * w - 32) * 9216 + n3;
#pragma unroll
        for (int s = 0; s < 18; ++s) {
            if (s == 4 || s == 13) { WAITV_V2(17 + (s < 13), hs[s == 4 ? 0 : 2], hs[s == 4 ? 1 : 3]); hf[s] = cat(hs[s == 4 ? 0 : 2], hs[s == 4 ? 1 : 3]); }
            else WAITV_V1(17 + (s < 4) + (s < 13), hf[s]);
            GL_INC(vwF, sW3, so3, lw, LA + s * 4096, 32 * 9216);
#pragma unroll
            for (int t = 0; t < 4; ++t) {
                const v4u wf = cat(wl[s & 1][t], wh[s & 1][t]);
                if (s == 0) MFMA_VV0(acc[t], wf, hf[s]); else MFMA_VV(acc[t], wf, hf[s]);
                if (s < 17) { wl[(s + 1) & 1][t] = trd(lap[4 + t] + (s + 1) * 4096); wh[(s + 1) & 1][t] = trd(lap[4 + t] + (s + 1) * 4096 + 2048); }
                SB();
            }
        }
        NOP8();
        asm volatile("" : "+v"(acc[0]), "+v"(acc[1]), "+v"(acc[2]), "+v"(acc[3]));
        TS(5);
        {
            float s1 = 0.f, s2 = 0.f;
            v4u o[2];
#pragma unroll
            for (int t = 0; t < 4; ++t) {
#pragma unroll
                for (int h2 = 0; h2 < 2; ++h2) {
                    const unsigned hv = pk2(acc[t][2 * h2], acc[t][2 * h2 + 1]);
                    const unsigned xv = xres[t][h2];
                    const unsigned y = pk2(blo(xv) + blo(hv), bhi(xv) + bhi(hv));
                    o[t >> 1][(t & 1) * 2 + h2] = y;
                    const float a0 = blo(y), a1 = bhi(y);
                    s1 += a0 + a1;
                    s2 = __builtin_fmaf(a0, a0, __builtin_fmaf(a1, a1, s2));
                }
            }
            ST4(o[0], vl16, sWS, WS_X1F + (tile * 18 + 2 * c9) * 1024, 0);
            ST4(o[1], vl16, sWS, WS_X1F + (tile * 18 + 2 * c9) * 1024, 1024);
            v2u st; st.x = __float_as_uint(xsum4(s1)); st.y = __float_as_uint(xsum4(s2));
            ST2(st, g == 0 ? vl8 : OOB, sWS, WS_ST + (tile * 9 + c9) * 128, 0);
            WAITV(0);
            ST2(tagR, lane < 8 ? lane * 1024 : OOB, sWS, fb + F2_OFF + fi, 0);
        }
        TS(6);
        BARL();
    } else {
        LOAD_EPOCHS();
        TAKE_EPOCHS(0);
        for (int i = 0; i < WARM_SLEEP; ++i) __builtin_amdgcn_s_sleep(2);
        const int wv = (j - 18) * 4 + w;
        v4u dummy = {0u, 0u, 0u, 0u};
#pragma unroll
        for (int i = 0; i < 12; ++i) { const int p = wv + 56 * i; LD4W(dummy, vl16, sW2, (p < 648 ? p : wv) * 1024); }
#pragma unroll
        for (int i = 0; i < 11; ++i) { const int p = wv + 56 * i; LD4W(dummy, vl16, sW3, (p < 576 ? p : wv) * 9216 + 576 * 2 * xcd); }
#pragma unroll
        for (int i = 0; i < 2; ++i) { const int p = wv + 56 * i; LD4W(dummy, r8 * 9216 + pos * 16, sW3, (p < 72 ? p : wv) * 8 * 9216 + (576 * xcd + 512) * 2); }
        unsigned so3 = (8 * w - 32) * 9216 + n3;
#pragma unroll
        for (int i = 0; i < 18; ++i) GL_INC(vwF, sW3, so3, lw, LA + i * 4096, 32 * 9216);
#pragma unroll
        for (int i = 0; i < 4; ++i) glds(vpF, sW3, HEAD_PIECE(i) * 32 * 9216 + n3c, L0 + LC + HEAD_PIECE(i) * 1024);
#pragma unroll
        for (int i = 0; i < 12; ++i) { const int p = wv + 56 * i; LD4W(dummy, vl16, sW4, 576 * 1152 * xcd + (p < 648 ? p : wv) * 1024); }
        TS(5);
        BARV(0);
        OPQ(dummy);
        TS(6);
    }

    asm volatile("" : "+s"(w));
    {
        unsigned lapw = L0 + LA + (g >> 1) * 1024 + (4 * (g & 1) + q) * 128 + ((w ^ ((2 * (g & 1) + (q >> 1)) & 3)) << 5) + (lane & 3) * 8;
        unsigned so3 = (8 * w - 32) * 9216 + n3 + 128;
#pragma unroll
        for (int P = 0; P < 2; ++P) {
            f32x4 cs;
            v2u cl[18], ch[18];
#pragma unroll
            for (int s = 0; s < 4; ++s) { cl[s] = trd(lapw + s * 4096); ch[s] = trd(lapw + s * 4096 + 2048); }
#pragma unroll
            for (int s = 0; s < 18; ++s) {
                if (P == 0) GL_INC(vwF, sW3, so3, lw, LB + s * 4096, 32 * 9216);
                if (s == 0) MFMA_VV0(cs, cat(cl[s], ch[s]), ones); else MFMA_VV(cs, cat(cl[s], ch[s]), ones);
                if (s + 4 < 18) { cl[s + 4] = trd(lapw + (s + 4) * 4096); ch[s + 4] = trd(lapw + (s + 4) * 4096 + 2048); }
                SB();
            }
            if (P == 0) { BARV(0); lapw += LB - LA; OPQ(lapw); }
            NOP8();
            OPQ(cs);
            if (m == 0) *(LAS f32x4*)(L0 + LM + 256 + P * 256 + w * 64 + g * 16) = cs;
        }
    }
    TS(17);
    poll((lane < 18 ? lane : 0) * 8, sWS, team * FT + F2_OFF + xcd * 1024 + w * 256, tagT, dead);
    TS(7);
    const int tile0 = team * 8 + 2 * w;
    v2u stp[2][3];
    {
        const unsigned vs = g * 128 + m * 8, vs2 = g == 0 ? 1024 + m * 8 : OOB;
#pragma unroll
        for (int mt = 0; mt < 2; ++mt) {
            LD2V(stp[mt][0], vs, sWS, WS_ST + (tile0 + mt) * 1152, 0);
            LD2V(stp[mt][1], vs, sWS, WS_ST + (tile0 + mt) * 1152, 512);
            LD2V(stp[mt][2], vs2, sWS, WS_ST + (tile0 + mt) * 1152, 0);
        }
    }
    v4u X[2][18];
    const unsigned x1_so = WS_X1F + tile0 * 18432;
#define LOAD_X(mt, s) LD4A(X[mt][s], vl16, sWS, x1_so + (mt)*18432 + ((s) >> 2) * 4096, ((s)&3) * 1024)
    constexpr int PF = 5;
#pragma unroll
    for (int s = 0; s < PF; ++s) { LOAD_X(0, s); LOAD_X(1, s); }
    v2u wl[8], wh[8];
#pragma unroll
    for (int t = 0; t < 8; ++t) { wl[t] = trd(lap[t]); wh[t] = trd(lap[t] + 2048); }
    float nmean[2], c1[2], c2[2], ia[2];
    asm volatile("s_waitcnt vmcnt(%6)" : "+v"(stp[0][0]), "+v"(stp[0][1]), "+v"(stp[0][2]), "+v"(stp[1][0]), "+v"(stp[1][1]), "+v"(stp[1][2]) : "i"(2 * PF));
#pragma unroll
    for (int mt = 0; mt < 2; ++mt) {
        const float s1 = xsum4(__uint_as_float(stp[mt][0].x) + __uint_as_float(stp[mt][1].x) + __uint_as_float(stp[mt][2].x));
        const float s2 = xsum4(__uint_as_float(stp[mt][0].y) + __uint_as_float(stp[mt][1].y) + __uint_as_float(stp[mt][2].y));
        const float mean = s1 * (1.0f / 576.0f);
        const float var = __builtin_fmaxf(__builtin_fmaf(-mean, mean, s2 * (1.0f / 576.0f)), 0.f) + 1e-5f;
        const float a = __builtin_amdgcn_rsqf(var);
        nmean[mt] = -mean; c1[mt] = a * GK1; c2[mt] = a * a * a * GK2; ia[mt] = var * a;
        asm volatile("" : "+v"(nmean[mt]), "+v"(c1[mt]), "+v"(c2[mt]), "+v"(ia[mt]));
    }
    f32x4 acc[2][8], accC[2], csC;
    SB();
    TS(8);
#pragma unroll
    for (int s = 0; s < 18; ++s) {
        WAITV_A2(s + PF <= 18 ? 2 * (PF - 1) : 2 * (17 - s), X[0][s], X[1][s]);
#pragma unroll
        for (int t = 0; t < 8; ++t) {
            const v4u wf = cat(wl[t], wh[t]);
            if (s == 0) MFMA_VA0(acc[0][t], wf, X[0][s]); else MFMA_VA(acc[0][t], wf, X[0][s]);
            if (s < 17 && t > 0) wh[t - 1] = trd(lap[t - 1] + (s + 1) * 4096 + 2048);
            if (s > 0 && t == 0) {}
            if (s == 0) MFMA_VA0(acc[1][t], wf, X[1][s]); else MFMA_VA(acc[1][t], wf, X[1][s]);
            if (s < 17) wl[t] = trd(lap[t] + (s + 1) * 4096);
            if (s < 17 && t == 7) wh[7] = trd(lap[7] + (s + 1) * 4096 + 2048);
            if (s + PF < 18 && t == 1) LOAD_X(0, s + PF);
            if (s + PF < 18 && t == 5) LOAD_X(1, s + PF);
            SB();
        }
    }
    BARL();
    TS(9);
    glds(vpF, sW3, (14 + w) * 32 * 9216 + n3c, lw + LA + 17 * 4096);
#define P2ADDR(s) (L0 + vl8 + ((s) < 14 ? LC + (s)*1024 : LA + 17 * 4096 + ((s)-14) * 1024))
    unsigned p2a = L0 + LC + vl8, p2b = L0 + LA + 17 * 4096 + vl8;
    OPQ(p2a); OPQ(p2b);
    const unsigned w4row = 576 * 1152 * xcd, n4c = (512 + 16 * sub) * 2;
    unsigned sb[5], vw4[4];
#pragma unroll
    for (int i = 0; i < 4; ++i) { sb[i] = w4row + (((sub + i) & 3) * 128 + 8 * w) * 1152 + 256 * sub; vw4[i] = vwD + i * 32 * 1152; }
    sb[4] = w4row + (512 + 8 * w) * 1152 + 256 * sub;
#define KPOS(u) ((u) < 16 ? ((4 * sub + (u)) & 15) : (u))
#define LOAD_Q(P, u) GL_Q(vw4[(u)&3], sW4, sb[(u) >> 2], lw, ((P) ? LB : LA) + (u)*4096, (P)*128)
    v4u own[2][4];
    const unsigned hh_so = WS_HH + (tile0 * 144 + 18 * xcd + 4 * sub) * 1024;
#pragma unroll
    for (int c = 0; c < 8; ++c) {
        const int tp = c >> 1, mt = c & 1;
        const f32x4 cs0 = *(LAS f32x4*)(L0 + LM + 256 + tp * 128 + g * 16), cs1 = *(LAS f32x4*)(L0 + LM + 256 + tp * 128 + 64 + g * 16);
#pragma unroll
        for (int i = 0; i < 3; ++i) { const int k = 3 * c + i; if (k < 17) LOAD_Q(0, k); else if (k > 17) LOAD_Q(1, k - 18); }
        own[mt][tp] = cat(gelu_acc(acc[mt][2 * tp], cs0, nmean[mt], c1[mt], c2[mt], ia[mt]), gelu_acc(acc[mt][2 * tp + 1], cs1, nmean[mt], c1[mt], c2[mt], ia[mt]));
        ST4(own[mt][tp], vl16, sWS, hh_so + mt * 147456, tp * 1024);
        SB();
    }
    BARV(31);
    TS(10);
    const unsigned f3_so = team * FT + F3_OFF + (xcd * 4 + w) * 128;
    const unsigned vf4 = (lane & 3) * 8;
    v2u fl;
#define P2RD(s, o) trd(((s) < 14 ? p2a + (s)*1024 : p2b + ((s)-14) * 1024) + (o))
#pragma unroll
    for (int t = 0; t < 4; ++t) { wl[t] = P2RD(t, 0); wh[t] = P2RD(t, 512); }
#pragma unroll
    for (int s = 0; s < 18; ++s) {
        if (s == 10) { WAITV(10); ST2(tagT, vone, sWS, f3_so + sub * 8, 0); }
        if (s < 12) LOAD_Q(1, s + 6);
        if (s == 14) LD2V(fl, vf4, sWS, f3_so, 0);
        const v4u wf = cat(wl[s & 3], wh[s & 3]);
        if (s == 0) { MFMA_VA0(accC[0], wf, X[0][s]); MFMA_VA0(accC[1], wf, X[1][s]); MFMA_VV0(csC, wf, ones); }
        else { MFMA_VA(accC[0], wf, X[0][s]); MFMA_VA(accC[1], wf, X[1][s]); MFMA_VV(csC, wf, ones); }
        if (s + 4 < 18) { wl[s & 3] = P2RD(s + 4, 0); wh[s & 3] = P2RD(s + 4, 512); }
        SB();
    }
    NOP8();
    asm volatile("" : "+v"(accC[0]), "+v"(accC[1]), "+v"(csC));
#pragma unroll
    for (int mt = 0; mt < 2; ++mt)
        ST2(gelu_acc(accC[mt], csC, nmean[mt], c1[mt], c2[mt], ia[mt]), vl8, sWS, WS_HH + ((tile0 + mt) * 144 + 18 * xcd + 16 + (sub >> 1)) * 1024 + (sub & 1) * 512, 0);
    TS(11);
    POLL_CHECK(2, fl, vf4, sWS, f3_so, tagT, dead);
    v4u Hf[2][12], Hc[2][2];
    v2u hc[2][2][2];
    const unsigned h_so = WS_HH + (tile0 * 144 + 18 * xcd) * 1024;
    unsigned hb[3];
#pragma unroll
    for (int i = 0; i < 3; ++i) hb[i] = h_so + ((sub + 1 + i) & 3) * 4096;
#define LOAD_H(mt, u) LD4A(Hf[mt][(u)-4], vl16, sWS, hb[((u) >> 2) - 1] + (mt)*147456, ((u)&3) * 1024)
#pragma unroll
    for (int u = 4; u < 8; ++u) { LOAD_H(0, u); LOAD_H(1, u); }
    BARL();
    TS(18);
#pragma unroll
    for (int i = 0; i < 4; ++i) glds(vwD, sW4, w4row + (17 * 32 + 8 * i) * 1152 + 256 * sub, L0 + LA + 17 * 4096 + i * 1024);
#pragma unroll
    for (int i = 0; i < 4; ++i) glds(vpD, sW4, w4row + KPOS(HEAD_PIECE(i)) * 32 * 1152 + n4c, L0 + LC + HEAD_PIECE(i) * 1024);
#pragma unroll
    for (int t = 0; t < 4; ++t) { wl[t] = trd(lap[t]); wh[t] = trd(lap[t] + 2048); }
    SB();
    const unsigned pp_so = WS_PP + (((team * 4 + sub) * 8 + 2 * w) * 8 + xcd) * 4608;
    const unsigned f4_so = team * FT + F4_OFF + (sub * 4 + w) * 256;
#define G4_MFMA(ac, mt, wf, s)                                                               \
    do {                                                                                     \
        if ((s) == 0) MFMA_VV0(ac, wf, own[mt][0]);                                          \
        else if ((s) < 4) MFMA_VV(ac, wf, own[mt][(s) < 4 ? (s) : 0]);                       \
        else if ((s) < 16) MFMA_VA(ac, wf, Hf[mt][(s) >= 4 && (s) < 16 ? (s)-4 : 0]);        \
        else MFMA_VV(ac, wf, Hc[mt][(s) >= 16 ? (s)-16 : 0]);                                \
    } while (0)
#define PART_STAGE(kk, P)                                                                    \
    do {                                                                                     \
        const int id_ = (kk) >> 1, mt_ = id_ >> 2, t_ = id_ & 3, e_ = ((kk)&1) * 2;          \
        po[(t_ & 1) * 2 + ((kk)&1)] = pk2(acc[mt_][4 * (P) + t_][e_], acc[mt_][4 * (P) + t_][e_ + 1]); \
        if (((kk)&3) == 3) ST4(po, vl16, sWS, pp_so + mt_ * 36864, (2 * (P) + (t_ >> 1)) * 1024); \
    } while (0)
    v4u po;
#pragma unroll
    for (int s = 0; s < 18; ++s) {
        if (s == 2) { WAITV(20); ST2(tagT, vone, sWS, f3_so + 32 + sub * 8, 0); }
        if (s >= 4 && s <= 10) WAITV_A2(s < 8 ? 23 : s == 8 ? 15 : s == 9 ? 14 : 11, Hf[0][s >= 4 && s <= 10 ? s - 4 : 0], Hf[1][s >= 4 && s <= 10 ? s - 4 : 0]);
        if (s == 8) LD2V(fl, vf4, sWS, f3_so, 32);
        if (s == 11) {
            POLL_CHECK(0, fl, vf4, sWS, f3_so + 32, tagT, dead);
#pragma unroll
            for (int u = 7; u < 12; ++u) asm volatile("" : "+a"(Hf[0][u]), "+a"(Hf[1][u]));
#pragma unroll
            for (int i = 0; i < 8; ++i) LD2V(hc[(i >> 1) & 1][i >> 2][i & 1], vl8, sWS, h_so + 16384 + ((i >> 1) & 1) * 147456, (i >> 2) * 1024 + (i & 1) * 512);
        }
        if (s >= 16) {
            const int u = s >= 16 ? s - 16 : 0;
            asm volatile("s_waitcnt vmcnt(%4)" : "+v"(hc[0][u][0]), "+v"(hc[0][u][1]), "+v"(hc[1][u][0]), "+v"(hc[1][u][1]) : "i"(s == 16 ? 4 : 0));
            Hc[0][u] = cat(hc[0][u][0], hc[0][u][1]); Hc[1][u] = cat(hc[1][u][0], hc[1][u][1]);
        }
#pragma unroll
        for (int t = 0; t < 4; ++t) {
            const v4u wf = cat(wl[t], wh[t]);
            G4_MFMA(acc[0][t], 0, wf, s);
            if (s < 17 && t > 0) wh[t - 1] = trd(lap[t - 1] + (s + 1) * 4096 + 2048);
            G4_MFMA(acc[1][t], 1, wf, s);
            if (s < 17) wl[t] = trd(lap[t] + (s + 1) * 4096);
            if (s < 17 && t == 3) wh[3] = trd(lap[3] + (s + 1) * 4096 + 2048);
            if (s == 17) { wl[t] = trd(lap[4 + t]); wh[t] = trd(lap[4 + t] + 2048); }
            if (s < 8 && t == 0) LOAD_H(0, s + 8);
            if (s < 8 && t == 2) LOAD_H(1, s + 8);
            SB();
        }
    }
    BARL();
    TS(12);
    glds(vpD, sW4, w4row + KPOS(14 + w) * 32 * 1152 + n4c, lw + LA);
#pragma unroll
    for (int s = 0; s < 18; ++s) {
        if (s == 10) { WAITV(0); ST2(tagT, vone, sWS, f4_so + xcd * 8, 0); }
#pragma unroll
        for (int t = 0; t < 4; ++t) {
            const v4u wf = cat(wl[t], wh[t]);
            G4_MFMA(acc[0][4 + t], 0, wf, s);
            if (s < 17 && t > 0) wh[t - 1] = trd(lap[3 + t] + (s + 1) * 4096 + 2048);
            if (s == 1 || s == 2) PART_STAGE((s - 1) * 8 + 2 * t, 0);
            G4_MFMA(acc[1][4 + t], 1, wf, s);
            if (s < 17) wl[t] = trd(lap[4 + t] + (s + 1) * 4096);
            if (s < 17 && t == 3) wh[3] = trd(lap[7] + (s + 1) * 4096 + 2048);
            if (s == 1 || s == 2) PART_STAGE((s - 1) * 8 + 2 * t + 1, 0);
            SB();
        }
    }
    BARL();
    TS(13);
    p2b = L0 + LA + vl8;
#pragma unroll
    for (int t = 0; t < 4; ++t) { wl[t] = P2RD(t, 0); wh[t] = P2RD(t, 512); }
#pragma unroll
    for (int kk = 0; kk < 16; ++kk) PART_STAGE(kk, 1);
    SB();
#pragma unroll
    for (int s = 0; s < 18; ++s) {
        const v4u wf = cat(wl[s & 3], wh[s & 3]);
        G4_MFMA(accC[0], 0, wf, s);
        G4_MFMA(accC[1], 1, wf, s);
        if (s + 4 < 18) { wl[s & 3] = P2RD(s + 4, 0); wh[s & 3] = P2RD(s + 4, 512); }
        SB();
    }
    NOP8();
    asm volatile("" : "+v"(accC[0]), "+v"(accC[1]));
#pragma unroll
    for (int mt = 0; mt < 2; ++mt) {
        v2u o; o.x = pk2(accC[mt][0], accC[mt][1]); o.y = pk2(accC[mt][2], accC[mt][3]);
        ST2(o, vl8, sWS, pp_so + mt * 36864 + 4096, 0);
    }
    TS(19);
    WAITV(2);
    ST2(tagT, vone, sWS, f4_so + 64 + xcd * 8, 0);
    WAITV(1);
    ST2(tagT, vone, sWS, f4_so + 128 + xcd * 8, 0);
    TS(14);
    {
        asm volatile("" : "+s"(w));
        const unsigned fr_so = team * FT + F4_OFF + (sub * 4 + (xcd >> 1)) * 256;
        const unsigned pr_so = WS_PP + (((team * 4 + sub) * 8 + xcd) * 8) * 4608;
        const int tileo = team * 8 + xcd;
        const unsigned vo = m * 1152 + g * 8;
        poll((lane & 7) * 8 + ((w == 0 && (lane & 8)) ? 128 : (w >> 1) * 64), sWS, fr_so, tagT, dead);
        v4u p[8], xr;
        v2u c[8], xc;
#pragma unroll
        for (int i = 0; i < 8; ++i) LD4V(p[i], vl16, sWS, pr_so + i * 4608 + w * 1024, 0);
        LD4V(xr, vl16, sWS, WS_X1F + (tileo * 18 + 4 * sub + w) * 1024, 0);
        const unsigned vc8 = w == 0 ? vl8 : OOB, vc16 = w == 0 ? vl16 : OOB, voc = w == 0 ? vo : OOB;
#pragma unroll
        for (int i = 0; i < 8; ++i) LD2V(c[i], vc8, sWS, pr_so + i * 4608 + 4096, 0);
        LD2V(xc, vc16, sWS, WS_X1F + (tileo * 18 + 16 + (sub >> 1)) * 1024 + (sub & 1) * 8, 0);
        asm volatile("s_waitcnt vmcnt(9)" : "+v"(p[0]), "+v"(p[1]), "+v"(p[2]), "+v"(p[3]), "+v"(p[4]), "+v"(p[5]), "+v"(p[6]), "+v"(p[7]), "+v"(xr));
        v4u o;
#pragma unroll
        for (int e = 0; e < 4; ++e) {
            float a0 = 0.f, a1 = 0.f;
#pragma unroll
            for (int i = 0; i < 8; ++i) { a0 += blo(p[i][e]); a1 += bhi(p[i][e]); }
            const unsigned hv = pk2(a0, a1);
            o[e] = pk2(blo(xr[e]) + blo(hv), bhi(xr[e]) + bhi(hv));
        }
        asm volatile("s_waitcnt vmcnt(0)" : "+v"(c[0]), "+v"(c[1]), "+v"(c[2]), "+v"(c[3]), "+v"(c[4]), "+v"(c[5]), "+v"(c[6]), "+v"(c[7]), "+v"(xc));
        v2u o0 = {o.x, o.y}, o1 = {o.z, o.w};
        ST2P(o0, vo, sO, tileo * 16 * 1152 + (128 * sub + 32 * w) * 2, 0);
        ST2P(o1, vo, sO, tileo * 16 * 1152 + (128 * sub + 32 * w) * 2, 32);
        v2u oc;
#pragma unroll
        for (int e = 0; e < 2; ++e) {
            float a0 = 0.f, a1 = 0.f;
#pragma unroll
            for (int i = 0; i < 8; ++i) { a0 += blo(c[i][e]); a1 += bhi(c[i][e]); }
            const unsigned hv = pk2(a0, a1);
            oc[e] = pk2(blo(xc[e]) + blo(hv), bhi(xc[e]) + bhi(hv));
        }
        ST2P(oc, voc, sO, tileo * 16 * 1152 + (512 + 16 * sub) * 2, 0);
        if (xcd == 0 && sub == 0 && w == 0) ST2(tagT, vone, sWS, team * FT, 0);
    }
}

extern "C" void kernel_launch(void* const* d_in, const int* in_sizes, int n_in, void* d_out, int out_size, void* d_ws, size_t ws_size,
                              hipStream_t stream) {
    mixer_kernel<<<256, 256, 0, stream>>>((const u16*)d_in[0], (const u16*)d_in[1], (const u16*)d_in[2], (const u16*)d_in[3],
                                          (const u16*)d_in[4], (u16*)d_out, (unsigned char*)d_ws);
}
```

```cpp
#include <hip/hip_runtime.h>
#include <stdint.h>

typedef unsigned short u16;
typedef unsigned v4u __attribute__((ext_vector_type(4)));
typedef unsigned v2u __attribute__((ext_vector_type(2)));
typedef short s16x4 __attribute__((ext_vector_type(4)));
typedef short bf16x8 __attribute__((ext_vector_type(8)));
typedef float f32x4 __attribute__((ext_vector_type(4)));
typedef float f32x2 __attribute__((ext_vector_type(2)));
typedef __bf16 bf16x2 __attribute__((ext_vector_type(2)));
#define LAS __attribute__((address_space(3)))
#define DI __device__ __forceinline__

#define WS_FLAG 0
#define WS_HF 262144
#define WS_X1F (WS_HF + 1179648)
#define WS_ST (WS_X1F + 1179648)
#define WS_HH (WS_ST + 73728)
#define WS_PP (WS_HH + 9437184)
#define WS_DBG (WS_PP + 9437184)
#define WS_TOTAL (WS_DBG + 131072)
constexpr int FT = 0x8000, F1_OFF = 0x100, F2_OFF = 0x800, F3_OFF = 0x2800, F4_OFF = 0x3800;
constexpr int LA = 0, LB = 73728, LC = 147456, LM = 161792;
constexpr int SPIN_CAP = 40000;
#define WARM_SLEEP 24

DI v4u mk_srd(const void* p, unsigned bytes) {
    unsigned long long a = (unsigned long long)p;
    v4u s; s.x = (unsigned)a; s.y = (unsigned)(a >> 32) & 0xffffu; s.z = bytes; s.w = 0x20000u; return s;
}
DI void glds(unsigned voff, v4u srd, unsigned soff, unsigned lds) {
    asm volatile("s_mov_b32 m0, %3\n\ts_nop 0\n\tbuffer_load_dwordx4 %0, %1, %2 offen lds" ::"v"(voff), "s"(srd), "s"(soff), "s"(lds));
}
#define LD4A(d, voff, srd, soff, imm) asm volatile("buffer_load_dwordx4 %0, %1, %2, %3 offen offset:%4 sc1" : "=a"(d) : "v"(voff), "s"(srd), "s"(soff), "i"(imm))
#define LD4V(d, voff, srd, soff, imm) asm volatile("buffer_load_dwordx4 %0, %1, %2, %3 offen offset:%4 sc1" : "=v"(d) : "v"(voff), "s"(srd), "s"(soff), "i"(imm))
#define LD2V(d, voff, srd, soff, imm) asm volatile("buffer_load_dwordx2 %0, %1, %2, %3 offen offset:%4 sc1" : "=v"(d) : "v"(voff), "s"(srd), "s"(soff), "i"(imm))
#define LD2A(d, voff, srd, soff, imm) asm volatile("buffer_load_dwordx2 %0, %1, %2, %3 offen offset:%4 sc1" : "=a"(d) : "v"(voff), "s"(srd), "s"(soff), "i"(imm))
#define LD4W(d, voff, srd, soff) asm volatile("buffer_load_dwordx4 %0, %1, %2, %3 offen" : "+v"(d) : "v"(voff), "s"(srd), "s"(soff))
#define ST4(d, voff, srd, soff, imm) asm volatile("buffer_store_dwordx4 %0, %1, %2, %3 offen offset:%4 sc1\n\ts_nop 1" ::"v"(d), "v"(voff), "s"(srd), "s"(soff), "i"(imm))
#define ST2(d, voff, srd, soff, imm) asm volatile("buffer_store_dwordx2 %0, %1, %2, %3 offen offset:%4 sc1" ::"v"(d), "v"(voff), "s"(srd), "s"(soff), "i"(imm))
#define ST2P(d, voff, srd, soff, imm) asm volatile("buffer_store_dwordx2 %0, %1, %2, %3 offen offset:%4" ::"v"(d), "v"(voff), "s"(srd), "s"(soff), "i"(imm))
#define WAITV(n) asm volatile("s_waitcnt vmcnt(%0)" ::"i"(n) : "memory")
#define WAITV_A2(n, a, b) asm volatile("s_waitcnt vmcnt(%2)" : "+a"(a), "+a"(b) : "i"(n))
#define WAITV_V2(n, a, b) asm volatile("s_waitcnt vmcnt(%2)" : "+v"(a), "+v"(b) : "i"(n))
#define WAITV_V1(n, a) asm volatile("s_waitcnt vmcnt(%1)" : "+v"(a) : "i"(n))
#define BARV(n) asm volatile("s_waitcnt vmcnt(%0) lgkmcnt(0)\n\ts_barrier" ::"i"(n) : "memory")
#define BARL() asm volatile("s_waitcnt lgkmcnt(0)\n\ts_barrier" ::: "memory")
#define SB() __builtin_amdgcn_sched_barrier(0)
#define MFMA_VA(c, a, b) asm volatile("v_mfma_f32_16x16x32_bf16 %0, %1, %2, %0" : "+v"(c) : "v"(a), "a"(b))
#define MFMA_VV(c, a, b) asm volatile("v_mfma_f32_16x16x32_bf16 %0, %1, %2, %0" : "+v"(c) : "v"(a), "v"(b))
#define MFMA_VV0(c, a, b) asm volatile("v_mfma_f32_16x16x32_bf16 %0, %1, %2, 0" : "=&v"(c) : "v"(a), "v"(b))
#define MFMA_VA0(c, a, b) asm volatile("v_mfma_f32_16x16x32_bf16 %0, %1, %2, 0" : "=&v"(c) : "v"(a), "a"(b))
#define GL_INC(voff, srd, so, lw, limm, sinc) asm volatile("s_add_u32 m0, %3, %4\n\ts_add_u32 %0, %0, %5\n\tbuffer_load_dwordx4 %1, %2, %0 offen lds" : "+s"(so) : "v"(voff), "s"(srd), "s"(lw), "i"(limm), "i"(sinc) : "scc")
#define GL_Q(voff, srd, so, lw, limm, off) asm volatile("s_add_u32 m0, %3, %4\n\ts_nop 0\n\tbuffer_load_dwordx4 %0, %1, %2 offen offset:%5 lds" ::"v"(voff), "s"(srd), "s"(so), "s"(lw), "i"((limm) - (off)), "i"(off) : "scc")
#define OPQ(x) asm volatile("" : "+v"(x))
#define NOP8() asm volatile("s_nop 7\n\ts_nop 1" ::: "memory")

DI v2u trd(unsigned addr) { return __builtin_bit_cast(v2u, __builtin_amdgcn_ds_read_tr16_b64_v4i16((LAS s16x4*)addr)); }
DI v4u cat(v2u a, v2u b) { v4u r; r.x = a.x; r.y = a.y; r.z = b.x; r.w = b.y; return r; }
DI f32x4 mfma_b(v4u a, v4u b, f32x4 c) {
    return __builtin_amdgcn_mfma_f32_16x16x32_bf16(__builtin_bit_cast(bf16x8, a), __builtin_bit_cast(bf16x8, b), c, 0, 0, 0);
}
DI unsigned pk2(float a, float b) { bf16x2 p; p.x = (__bf16)a; p.y = (__bf16)b; return __builtin_bit_cast(unsigned, p); }
DI float blo(unsigned u) { return __uint_as_float(u << 16); }
DI float bhi(unsigned u) { return __uint_as_float(u & 0xffff0000u); }
DI float xsum4(float x) {
    auto s = __builtin_amdgcn_permlane16_swap(__float_as_uint(x), __float_as_uint(x), false, false);
    x = __uint_as_float(s[0]) + __uint_as_float(s[1]);
    auto t = __builtin_amdgcn_permlane32_swap(__float_as_uint(x), __float_as_uint(x), false, false);
    return __uint_as_float(t[0]) + __uint_as_float(t[1]);
}
constexpr float GK1 = -2.3022081986f, GK2 = -0.1029432397f;
DI float gelu1(float z) {
    float e = __builtin_amdgcn_exp2f(z * __builtin_fmaf(z * z, GK2, GK1));
    return z * __builtin_amdgcn_rcpf(1.0f + e);
}
DI void poll(unsigned voff, v4u srd, unsigned soff, v2u tag, int& dead) {
    if (dead) return;
    for (int it = 0;; ++it) {
        v2u f;
        asm volatile("buffer_load_dwordx2 %0, %1, %2, %3 offen sc1\n\ts_waitcnt vmcnt(0)" : "=&v"(f) : "v"(voff), "s"(srd), "s"(soff) : "memory");
        if (__all(f.x == tag.x && f.y == tag.y)) return;
        if (it > SPIN_CAP) { dead = 1; return; }
        __builtin_amdgcn_s_sleep(1);
    }
}
#define POLL_CHECK(n, f, voff, srd, soff, tag, dead) \
    do { WAITV_V1(n, f); if (!__all(f.x == tag.x && f.y == tag.y)) poll(voff, srd, soff, tag, dead); } while (0)

#define TS(i) do {} while (0)

DI unsigned gelu_pair(f32x2 v, float c1, float c2, float ia) {
    const f32x2 t = v * __builtin_elementwise_fma(v * v, f32x2{c2, c2}, f32x2{c1, c1});
    f32x2 e; e.x = __builtin_amdgcn_exp2f(t.x); e.y = __builtin_amdgcn_exp2f(t.y);
    const f32x2 d = __builtin_elementwise_fma(e, f32x2{ia, ia}, f32x2{ia, ia});
    f32x2 r; r.x = __builtin_amdgcn_rcpf(d.x); r.y = __builtin_amdgcn_rcpf(d.y);
    const f32x2 y = v * r;
    return pk2(y.x, y.y);
}
DI v2u gelu_acc(f32x4 a, f32x4 cs, float nm, float c1, float c2, float ia) {
    const f32x4 v = __builtin_elementwise_fma(cs, f32x4{nm, nm, nm, nm}, a);
    v2u o; o.x = gelu_pair(f32x2{v[0], v[1]}, c1, c2, ia); o.y = gelu_pair(f32x2{v[2], v[3]}, c1, c2, ia);
    return o;
}

__global__ __launch_bounds__(256, 1) void mixer_kernel(const u16* __restrict__ x, const u16* __restrict__ w1, const u16* __restrict__ w2,
                                                       const u16* __restrict__ w3, const u16* __restrict__ w4, u16* __restrict__ out,
                                                       unsigned char* __restrict__ ws) {
    __shared__ __attribute__((aligned(1024))) unsigned char smem[163840];
    TS(0);
    const int b = blockIdx.x, xcd = b & 7, j = b >> 3, team = j & 7, sub = j >> 3;
    const int lane = threadIdx.x & 63;
    int w = __builtin_amdgcn_readfirstlane(threadIdx.x >> 6);
    const int m = lane & 15, g = lane >> 4, q = m >> 2, r8 = lane >> 3, pos = lane & 7;
    const unsigned L0 = (unsigned)(size_t)(LAS unsigned char*)smem, lw = L0 + w * 1024;
    const v4u sX = mk_srd(x, 1179648), sW1 = mk_srd(w1, 663552), sW2 = mk_srd(w2, 663552), sW3 = mk_srd(w3, 5308416),
              sW4 = mk_srd(w4, 5308416), sO = mk_srd(out, 1179648), sWS = mk_srd(ws, WS_TOTAL);
    const unsigned vl16 = lane * 16, vl8 = lane * 8;
    constexpr unsigned OOB = 0x80000000u;
    const unsigned vone = lane == 0 ? 0u : OOB;
    const unsigned swzc = (((pos >> 1) ^ ((r8 >> 1) & 3)) << 5) + (pos & 1) * 16;
    const unsigned vwD = r8 * 1152 + swzc, vwF = r8 * 9216 + swzc;
    const unsigned vpD = (lane >> 1) * 1152 + (lane & 1) * 16, vpF = (lane >> 1) * 9216 + (lane & 1) * 16;
    const unsigned n3 = (576 * xcd + 128 * sub) * 2, n3c = (576 * xcd + 512 + 16 * sub) * 2;
    const bool act = j < 18;
    v4u junk = {0u, 0u, 0u, 0u};
    {
        unsigned long long pc; unsigned len;
        asm volatile("s_getpc_b64 %0\n.Lmx_here:\n\ts_mov_b32 %1, .Lmx_end-.Lmx_here" : "=s"(pc), "=s"(len));
        v4u sC; sC.x = (unsigned)pc; sC.y = (unsigned)(pc >> 32) & 0xffffu; sC.z = len; sC.w = 0x20000u;
        LD4W(junk, vl16, sC, (j * 4 + w) * 1024);
    }
    {
        const int wv = j * 4 + w;
#pragma unroll
        for (int i = 0; i < 5; ++i) LD4W(junk, vl16, sW1, ((wv + 128 * i + 81 * xcd) % 648) * 1024);
        LD4W(junk, vl16, sW1, wv < 8 ? ((wv + 640 + 81 * xcd) % 648) * 1024 : OOB);
        LD4W(junk, vl16, sX, xcd * 147456 + wv * 1024);
        LD4W(junk, vl16, sX, wv < 16 ? xcd * 147456 + (128 + wv) * 1024 : OOB);
    }
    int dead = 0;
    v2u tagR, tagT;
    unsigned lap[8];
    {
        const int r4 = 4 * (g & 1) + q, xp = (r4 >> 1) & 3;
#pragma unroll
        for (int t = 0; t < 4; ++t) {
            lap[t] = L0 + LA + (g >> 1) * 1024 + r4 * 128 + ((t ^ xp) << 5) + (lane & 3) * 8;
            lap[4 + t] = lap[t] + (LB - LA);
            OPQ(lap[4 + t]);
        }
    }
    v4u ones = {0x3f803f80u, 0x3f803f80u, 0x3f803f80u, 0x3f803f80u};
    OPQ(ones);
#define LOAD_EPOCHS() do { LD2V(tagR, 0u, sWS, xcd * FT, 0); LD2V(tagT, 0u, sWS, team * FT, 0); } while (0)
#define TAKE_EPOCHS(n) do { WAITV_V2(n, tagR, tagT); tagR.x += 1; tagT.x += 1; } while (0)
#define HEAD_PIECE(i) ((w + 4 * (i) < 14) ? w + 4 * (i) : w + 8)

    if (act) {
        const int rh = (j * 29) >> 8, c9 = j - 9 * rh, tok0 = xcd * 128 + rh * 64;
        const unsigned vx0 = r8 * 1152 + ((pos ^ (r8 >> 1)) << 4), vx1 = r8 * 1152 + ((pos ^ ((r8 >> 1) | 4)) << 4);
        const unsigned vw1 = r8 * 1152 + (((pos >> 1) ^ (((r8 >> 1) & 1) | ((w & 1) << 1))) << 5) + (pos & 1) * 16;
#define G1_LINE(u) (c9 + (u) >= 9 ? c9 + (u)-9 : c9 + (u))
#define G1_X(u) do { glds(vx0, sX, (tok0 + 16 * w) * 1152 + G1_LINE(u) * 128, L0 + LB + (2 * w * 9 + (u)) * 1024); \
                     glds(vx1, sX, (tok0 + 16 * w + 8) * 1152 + G1_LINE(u) * 128, L0 + LB + ((2 * w + 1) * 9 + (u)) * 1024); } while (0)
#define G1_W(u) do { glds(vw1, sW1, (G1_LINE(u) * 64 + 8 * w) * 1152 + c9 * 128, lw + LA + (u)*8192); \
                     glds(vw1, sW1, (G1_LINE(u) * 64 + 8 * w + 32) * 1152 + c9 * 128, lw + LA + (u)*8192 + 4096); } while (0)
        LOAD_EPOCHS();
#pragma unroll
        for (int u = 0; u < 6; ++u) { G1_X(u); G1_W(u); }
        TAKE_EPOCHS(24);
        const int swz = (m >> 1) & 7;
        const unsigned xrow = L0 + LB + (2 * w + (m >> 3)) * 9216 + (m & 7) * 128;
        unsigned xa0 = xrow + ((g ^ swz) << 4), xa1 = xrow + ((g ^ swz ^ 4) << 4);
        OPQ(xa0); OPQ(xa1);
        unsigned la1[4];
        {
            const int x1 = (q >> 1) | ((g & 1) << 1);
#pragma unroll
            for (int t = 0; t < 4; ++t) la1[t] = L0 + LA + g * 1024 + q * 128 + ((t ^ w ^ x1) << 5) + (lane & 3) * 8;
        }
        f32x4 acc[4], acs, asum;
        float ssq = 0.f;
        v2u xres[4], wl[2][4], wh[2][4];
        v4u xf[2];
#define G1_RDX(s) xf[(s)&1] = *(LAS v4u*)(((s)&1 ? xa1 : xa0) + ((s) >> 1) * 1024)
#define G1_RDL(s, t) wl[(s)&1][t] = trd(la1[t] + (s)*4096)
#define G1_RDH(s, t) wh[(s)&1][t] = trd(la1[t] + (s)*4096 + 512)
#pragma unroll
        for (int s = 0; s < 18; ++s) {
            if (s == 0) { BARV(12); OPQ(junk); TS(1); }
            if (s == 6) BARV(12);
            if (s == 12) BARV(4);
            if (s == 0 || s == 6 || s == 12) {
                G1_RDX(s);
#pragma unroll
                for (int t = 0; t < 4; ++t) { G1_RDL(s, t); G1_RDH(s, t); }
            }
            if (s == 0) {
#pragma unroll
                for (int t = 0; t < 4; ++t) xres[t] = *(LAS v2u*)(xrow + (((2 * t + (g >> 1)) ^ swz) << 4) + (g & 1) * 8);
            }
            if (s < 6) { if (s & 1) G1_W(6 + (s >> 1)); else G1_X(6 + (s >> 1)); }
            else if (s < 10) glds(vpF, sW3, HEAD_PIECE(s - 6) * 32 * 9216 + n3c, L0 + LC + HEAD_PIECE(s - 6) * 1024);
            const bool nx = s != 5 && s != 11 && s != 17;
            const v4u xc = xf[s & 1];
            if (nx) G1_RDX(s + 1);
#pragma unroll
            for (int t = 0; t < 4; ++t) {
                const v4u wf = cat(wl[s & 1][t], wh[s & 1][t]);
                if (s == 0) MFMA_VV0(acc[t], wf, xc); else MFMA_VV(acc[t], wf, xc);
                if (nx) G1_RDL(s + 1, t);
                if (t == 0) { if (s == 0) MFMA_VV0(acs, wf, ones); else MFMA_VV(acs, wf, ones); }
                if (t == 1) { if (s == 0) MFMA_VV0(asum, ones, xc); else MFMA_VV(asum, ones, xc); }
                if (nx) G1_RDH(s + 1, t);
                const unsigned xe = xc[t];
                ssq = __builtin_amdgcn_fdot2_f32_bf16(__builtin_bit_cast(bf16x2, xe), __builtin_bit_cast(bf16x2, xe), ssq, false);
                SB();
            }
        }
        NOP8();
        asm volatile("" : "+v"(acs), "+v"(asum), "+v"(acc[0]), "+v"(acc[1]), "+v"(acc[2]), "+v"(acc[3]));
        if (m == 0) *(LAS f32x4*)(L0 + LM + w * 64 + g * 16) = acs;
        BARL();
        TS(2);
        const unsigned fb = xcd * FT + (rh * 2 + (w >> 1)) * 256, fi = (c9 * 2 + (w & 1)) * 8;
        {
            const float mean = asum[0] * (1.0f / 576.0f);
            const float var = __builtin_fmaxf(__builtin_fmaf(-mean, mean, xsum4(ssq) * (1.0f / 576.0f)), 0.f) + 1e-5f;
            const float a = __builtin_amdgcn_rsqf(var);
            const int tok = 16 * (w & 1) + m, batch = xcd * 4 + rh * 2 + (w >> 1);
            v2u o[4];
#pragma unroll
            for (int t = 0; t < 4; ++t) o[t] = gelu_acc(acc[t], *(LAS f32x4*)(L0 + LM + (t ^ w) * 64 + g * 16), -mean, a * GK1, a * a * a * GK2, var * a);
#pragma unroll
            for (int pp = 0; pp < 2; ++pp) {
                const v2u lo = (w & 1) ? o[2 * pp + 1] : o[2 * pp], hi = (w & 1) ? o[2 * pp] : o[2 * pp + 1];
                const int Te = 4 * c9 + 2 * ((w >> 1) ^ pp);
                const unsigned so = WS_HF + (batch * 36 + (Te >> 1)) * 1024;
                const int sh = Te < 9 ? 1 : Te < 18 ? -1 : Te < 27 ? 2 : -2, dest = tok + sh;
                const bool in = dest >= 0 && dest < 32;
                if (Te == 8 || Te == 26) {
                    const int d2 = tok - sh;
                    const bool in2 = d2 >= 0 && d2 < 32;
                    ST2((in ? lo : v2u{0u, 0u}), ((dest >> 4) & 1) * 18432 + g * 128 + (dest & 15) * 8, sWS, so, 0);
                    ST2((in2 ? hi : v2u{0u, 0u}), ((d2 >> 4) & 1) * 18432 + g * 128 + (d2 & 15) * 8, sWS, so, 512);
                } else {
                    ST4((in ? cat(lo, hi) : v4u{0u, 0u, 0u, 0u}), ((dest >> 4) & 1) * 18432 + g * 256 + (dest & 15) * 16, sWS, so, 0);
                }
            }
            TS(16);
            WAITV(0);
            ST2(tagR, vone, sWS, fb + F1_OFF + fi, 0);
        }
        TS(3);
        {
            unsigned so = (8 * w - 32) * 1152 + c9 * 128;
#pragma unroll
            for (int i = 0; i < 18; ++i) GL_INC(vwD, sW2, so, lw, LB + i * 4096, 32 * 1152);
        }
        poll((lane < 18 ? lane : 0) * 8, sWS, fb + F1_OFF, tagR, dead);
        TS(4);
        const int tile = xcd * 8 + rh * 4 + w;
        v4u hf[18];
        v2u hs[4];
#pragma unroll
        for (int s = 0; s < 18; ++s) {
            const unsigned so = WS_HF + (tile * 18 + (s & ~3)) * 1024;
            if (s == 4 || s == 13) { LD2V(hs[s == 4 ? 0 : 2], vl8, sWS, so, (s & 3) * 1024); LD2V(hs[s == 4 ? 1 : 3], vl8, sWS, so, (s & 3) * 1024 + 512); }
            else LD4V(hf[s], vl16, sWS, so, (s & 3) * 1024);
        }
        BARL();
#pragma unroll
        for (int t = 0; t < 4; ++t) { wl[0][t] = trd(lap[4 + t]); wh[0][t] = trd(lap[4 + t] + 2048); }
        unsigned so3 = (8 * w - 32) * 9216 + n3;
#pragma unroll
        for (int s = 0; s < 18; ++s) {
            if (s == 4 || s == 13) { WAITV_V2(17 + (s < 13), hs[s == 4 ? 0 : 2], hs[s == 4 ? 1 : 3]); hf[s] = cat(hs[s == 4 ? 0 : 2], hs[s == 4 ? 1 : 3]); }
            else WAITV_V1(17 + (s < 4) + (s < 13), hf[s]);
            GL_INC(vwF, sW3, so3, lw, LA + s * 4096, 32 * 9216);
#pragma unroll
            for (int t = 0; t < 4; ++t) {
                const v4u wf = cat(wl[s & 1][t], wh[s & 1][t]);
                if (s == 0) MFMA_VV0(acc[t], wf, hf[s]); else MFMA_VV(acc[t], wf, hf[s]);
                if (s < 17) { wl[(s + 1) & 1][t] = trd(lap[4 + t] + (s + 1) * 4096); wh[(s + 1) & 1][t] = trd(lap[4 + t] + (s + 1) * 4096 + 2048); }
                SB();
            }
        }
        NOP8();
        asm volatile("" : "+v"(acc[0]), "+v"(acc[1]), "+v"(acc[2]), "+v"(acc[3]));
        TS(5);
        {
            float s1 = 0.f, s2 = 0.f;
            v4u o[2];
#pragma unroll
            for (int t = 0; t < 4; ++t) {
#pragma unroll
                for (int h2 = 0; h2 < 2; ++h2) {
                    const unsigned hv = pk2(acc[t][2 * h2], acc[t][2 * h2 + 1]);
                    const unsigned xv = xres[t][h2];
                    const unsigned y = pk2(blo(xv) + blo(hv), bhi(xv) + bhi(hv));
                    o[t >> 1][(t & 1) * 2 + h2] = y;
                    const float a0 = blo(y), a1 = bhi(y);
                    s1 += a0 + a1;
                    s2 = __builtin_fmaf(a0, a0, __builtin_fmaf(a1, a1, s2));
                }
            }
            ST4(o[0], vl16, sWS, WS_X1F + (tile * 18 + 2 * c9) * 1024, 0);
            ST4(o[1], vl16, sWS, WS_X1F + (tile * 18 + 2 * c9) * 1024, 1024);
            v2u st; st.x = __float_as_uint(xsum4(s1)); st.y = __float_as_uint(xsum4(s2));
            ST2(st, g == 0 ? vl8 : OOB, sWS, WS_ST + (tile * 9 + c9) * 128, 0);
            WAITV(0);
            ST2(tagR, lane < 8 ? lane * 1024 : OOB, sWS, fb + F2_OFF + fi, 0);
        }
        TS(6);
        BARL();
    } else {
        LOAD_EPOCHS();
        {
            unsigned d1 = 0;
            if (j < 20) {
#pragma unroll
                for (int i = 0; i < 1; ++i) asm volatile("buffer_load_dword %0, %1, %2, %3 offen" : "+v"(d1) : "v"(lane * 65536u), "s"(sWS), "s"((((j - 18) * 4 + w) * 64) * 65536u));
            } else if (j == 20 && w == 0) asm volatile("buffer_load_dword %0, %1, %2, 0 offen" : "+v"(d1) : "v"(lane * 65536u), "s"(sO));
            WAITV(0);
            OPQ(d1);
        }
        TAKE_EPOCHS(0);
        for (int i = 0; i < WARM_SLEEP; ++i) __builtin_amdgcn_s_sleep(2);
        const int wv = (j - 18) * 4 + w;
        v4u dummy = junk;
#pragma unroll
        for (int i = 0; i < 12; ++i) { const int p = wv + 56 * i; LD4W(dummy, vl16, sW2, (p < 648 ? p : wv) * 1024); }
#pragma unroll
        for (int i = 0; i < 11; ++i) { const int p = wv + 56 * i; LD4W(dummy, vl16, sW3, (p < 576 ? p : wv) * 9216 + 576 * 2 * xcd); }
#pragma unroll
        for (int i = 0; i < 2; ++i) { const int p = wv + 56 * i; LD4W(dummy, r8 * 9216 + pos * 16, sW3, (p < 72 ? p : wv) * 8 * 9216 + (576 * xcd + 512) * 2); }
        unsigned so3 = (8 * w - 32) * 9216 + n3;
#pragma unroll
        for (int i = 0; i < 18; ++i) GL_INC(vwF, sW3, so3, lw, LA + i * 4096, 32 * 9216);
#pragma unroll
        for (int i = 0; i < 4; ++i) glds(vpF, sW3, HEAD_PIECE(i) * 32 * 9216 + n3c, L0 + LC + HEAD_PIECE(i) * 1024);
#pragma unroll
        for (int i = 0; i < 12; ++i) { const int p = wv + 56 * i; LD4W(dummy, vl16, sW4, 576 * 1152 * xcd + (p < 648 ? p : wv) * 1024); }
        TS(5);
        BARV(0);
        OPQ(dummy);
        TS(6);
    }

    asm volatile("" : "+s"(w));
    {
        unsigned lapw = L0 + LA + (g >> 1) * 1024 + (4 * (g & 1) + q) * 128 + ((w ^ ((2 * (g & 1) + (q >> 1)) & 3)) << 5) + (lane & 3) * 8;
        unsigned so3 = (8 * w - 32) * 9216 + n3 + 128;
#pragma unroll
        for (int P = 0; P < 2; ++P) {
            f32x4 cs;
            v2u cl[18], ch[18];
#pragma unroll
            for (int s = 0; s < 4; ++s) { cl[s] = trd(lapw + s * 4096); ch[s] = trd(lapw + s * 4096 + 2048); }
#pragma unroll
            for (int s = 0; s < 18; ++s) {
                if (P == 0) GL_INC(vwF, sW3, so3, lw, LB + s * 4096, 32 * 9216);
                if (s == 0) MFMA_VV0(cs, cat(cl[s], ch[s]), ones); else MFMA_VV(cs, cat(cl[s], ch[s]), ones);
                if (s + 4 < 18) { cl[s + 4] = trd(lapw + (s + 4) * 4096); ch[s + 4] = trd(lapw + (s + 4) * 4096 + 2048); }
                SB();
            }
            if (P == 0) { BARV(0); lapw += LB - LA; OPQ(lapw); }
            NOP8();
            OPQ(cs);
            if (m == 0) *(LAS f32x4*)(L0 + LM + 256 + P * 256 + w * 64 + g * 16) = cs;
        }
    }
    TS(17);
    poll((lane < 18 ? lane : 0) * 8, sWS, team * FT + F2_OFF + xcd * 1024 + w * 256, tagT, dead);
    TS(7);
    const int tile0 = team * 8 + 2 * w;
    v2u stp[2][3];
    {
        const unsigned vs = g * 128 + m * 8, vs2 = g == 0 ? 1024 + m * 8 : OOB;
#pragma unroll
        for (int mt = 0; mt < 2; ++mt) {
            LD2V(stp[mt][0], vs, sWS, WS_ST + (tile0 + mt) * 1152, 0);
            LD2V(stp[mt][1], vs, sWS, WS_ST + (tile0 + mt) * 1152, 512);
            LD2V(stp[mt][2], vs2, sWS, WS_ST + (tile0 + mt) * 1152, 0);
        }
    }
    v4u X[2][18];
    const unsigned x1_so = WS_X1F + tile0 * 18432;
#define LOAD_X(mt, s) LD4A(X[mt][s], vl16, sWS, x1_so + (mt)*18432 + ((s) >> 2) * 4096, ((s)&3) * 1024)
    constexpr int PF = 5;
#pragma unroll
    for (int s = 0; s < PF; ++s) { LOAD_X(0, s); LOAD_X(1, s); }
    v2u wl[8], wh[8];
#pragma unroll
    for (int t = 0; t < 8; ++t) { wl[t] = trd(lap[t]); wh[t] = trd(lap[t] + 2048); }
    float nmean[2], c1[2], c2[2], ia[2];
    asm volatile("s_waitcnt vmcnt(%6)" : "+v"(stp[0][0]), "+v"(stp[0][1]), "+v"(stp[0][2]), "+v"(stp[1][0]), "+v"(stp[1][1]), "+v"(stp[1][2]) : "i"(2 * PF));
#pragma unroll
    for (int mt = 0; mt < 2; ++mt) {
        const float s1 = xsum4(__uint_as_float(stp[mt][0].x) + __uint_as_float(stp[mt][1].x) + __uint_as_float(stp[mt][2].x));
        const float s2 = xsum4(__uint_as_float(stp[mt][0].y) + __uint_as_float(stp[mt][1].y) + __uint_as_float(stp[mt][2].y));
        const float mean = s1 * (1.0f / 576.0f);
        const float var = __builtin_fmaxf(__builtin_fmaf(-mean, mean, s2 * (1.0f / 576.0f)), 0.f) + 1e-5f;
        const float a = __builtin_amdgcn_rsqf(var);
        nmean[mt] = -mean; c1[mt] = a * GK1; c2[mt] = a * a * a * GK2; ia[mt] = var * a;
        asm volatile("" : "+v"(nmean[mt]), "+v"(c1[mt]), "+v"(c2[mt]), "+v"(ia[mt]));
    }
    f32x4 acc[2][8], accC[2], csC;
    SB();
    TS(8);
#pragma unroll
    for (int s = 0; s < 18; ++s) {
        WAITV_A2(s + PF <= 18 ? 2 * (PF - 1) : 2 * (17 - s), X[0][s], X[1][s]);
#pragma unroll
        for (int t = 0; t < 8; ++t) {
            const v4u wf = cat(wl[t], wh[t]);
            if (s == 0) MFMA_VA0(acc[0][t], wf, X[0][s]); else MFMA_VA(acc[0][t], wf, X[0][s]);
            if (s < 17 && t > 0) wh[t - 1] = trd(lap[t - 1] + (s + 1) * 4096 + 2048);
            if (s > 0 && t == 0) {}
            if (s == 0) MFMA_VA0(acc[1][t], wf, X[1][s]); else MFMA_VA(acc[1][t], wf, X[1][s]);
            if (s < 17) wl[t] = trd(lap[t] + (s + 1) * 4096);
            if (s < 17 && t == 7) wh[7] = trd(lap[7] + (s + 1) * 4096 + 2048);
            if (s + PF < 18 && t == 1) LOAD_X(0, s + PF);
            if (s + PF < 18 && t == 5) LOAD_X(1, s + PF);
            SB();
        }
    }
    BARL();
    TS(9);
    glds(vpF, sW3, (14 + w) * 32 * 9216 + n3c, lw + LA + 17 * 4096);
#define P2ADDR(s) (L0 + vl8 + ((s) < 14 ? LC + (s)*1024 : LA + 17 * 4096 + ((s)-14) * 1024))
    unsigned p2a = L0 + LC + vl8, p2b = L0 + LA + 17 * 4096 + vl8;
    OPQ(p2a); OPQ(p2b);
    const unsigned w4row = 576 * 1152 * xcd, n4c = (512 + 16 * sub) * 2;
    unsigned sb[5], vw4[4];
#pragma unroll
    for (int i = 0; i < 4; ++i) { sb[i] = w4row + (((sub + i) & 3) * 128 + 8 * w) * 1152 + 256 * sub; vw4[i] = vwD + i * 32 * 1152; }
    sb[4] = w4row + (512 + 8 * w) * 1152 + 256 * sub;
#define KPOS(u) ((u) < 16 ? ((4 * sub + (u)) & 15) : (u))
#define LOAD_Q(P, u) GL_Q(vw4[(u)&3], sW4, sb[(u) >> 2], lw, ((P) ? LB : LA) + (u)*4096, (P)*128)
    v4u own[2][4];
    const unsigned hh_so = WS_HH + (tile0 * 144 + 18 * xcd + 4 * sub) * 1024;
#pragma unroll
    for (int c = 0; c < 8; ++c) {
        const int tp = c >> 1, mt = c & 1;
        const f32x4 cs0 = *(LAS f32x4*)(L0 + LM + 256 + tp * 128 + g * 16), cs1 = *(LAS f32x4*)(L0 + LM + 256 + tp * 128 + 64 + g * 16);
#pragma unroll
        for (int i = 0; i < 3; ++i) { const int k = 3 * c + i; if (k < 17) LOAD_Q(0, k); else if (k > 17) LOAD_Q(1, k - 18); }
        own[mt][tp] = cat(gelu_acc(acc[mt][2 * tp], cs0, nmean[mt], c1[mt], c2[mt], ia[mt]), gelu_acc(acc[mt][2 * tp + 1], cs1, nmean[mt], c1[mt], c2[mt], ia[mt]));
        ST4(own[mt][tp], vl16, sWS, hh_so + mt * 147456, tp * 1024);
        SB();
    }
    BARV(31);
    TS(10);
    const unsigned f3_so = team * FT + F3_OFF + (xcd * 4 + w) * 128;
    const unsigned vf4 = (lane & 3) * 8;
    v2u fl;
#define P2RD(s, o) trd(((s) < 14 ? p2a + (s)*1024 : p2b + ((s)-14) * 1024) + (o))
#pragma unroll
    for (int t = 0; t < 4; ++t) { wl[t] = P2RD(t, 0); wh[t] = P2RD(t, 512); }
#pragma unroll
    for (int s = 0; s < 18; ++s) {
        if (s == 10) { WAITV(10); ST2(tagT, vone, sWS, f3_so + sub * 8, 0); }
        if (s < 12) LOAD_Q(1, s + 6);
        if (s == 14) LD2V(fl, vf4, sWS, f3_so, 0);
        const v4u wf = cat(wl[s & 3], wh[s & 3]);
        if (s == 0) { MFMA_VA0(accC[0], wf, X[0][s]); MFMA_VA0(accC[1], wf, X[1][s]); MFMA_VV0(csC, wf, ones); }
        else { MFMA_VA(accC[0], wf, X[0][s]); MFMA_VA(accC[1], wf, X[1][s]); MFMA_VV(csC, wf, ones); }
        if (s + 4 < 18) { wl[s & 3] = P2RD(s + 4, 0); wh[s & 3] = P2RD(s + 4, 512); }
        SB();
    }
    NOP8();
    asm volatile("" : "+v"(accC[0]), "+v"(accC[1]), "+v"(csC));
#pragma unroll
    for (int mt = 0; mt < 2; ++mt)
        ST2(gelu_acc(accC[mt], csC, nmean[mt], c1[mt], c2[mt], ia[mt]), vl8, sWS, WS_HH + ((tile0 + mt) * 144 + 18 * xcd + 16 + (sub >> 1)) * 1024 + (sub & 1) * 512, 0);
    TS(11);
    POLL_CHECK(2, fl, vf4, sWS, f3_so, tagT, dead);
    v4u Hf[2][12], Hc[2][2];
    v2u hc[2][2][2];
    const unsigned h_so = WS_HH + (tile0 * 144 + 18 * xcd) * 1024;
    unsigned hb[3];
#pragma unroll
    for (int i = 0; i < 3; ++i) hb[i] = h_so + ((sub + 1 + i) & 3) * 4096;
#define LOAD_H(mt, u) LD4A(Hf[mt][(u)-4], vl16, sWS, hb[((u) >> 2) - 1] + (mt)*147456, ((u)&3) * 1024)
#pragma unroll
    for (int u = 4; u < 8; ++u) { LOAD_H(0, u); LOAD_H(1, u); }
    BARL();
    TS(18);
#pragma unroll
    for (int i = 0; i < 4; ++i) glds(vwD, sW4, w4row + (17 * 32 + 8 * i) * 1152 + 256 * sub, L0 + LA + 17 * 4096 + i * 1024);
#pragma unroll
    for (int i = 0; i < 4; ++i) glds(vpD, sW4, w4row + KPOS(HEAD_PIECE(i)) * 32 * 1152 + n4c, L0 + LC + HEAD_PIECE(i) * 1024);
#pragma unroll
    for (int t = 0; t < 4; ++t) { wl[t] = trd(lap[t]); wh[t] = trd(lap[t] + 2048); }
    SB();
    const unsigned pp_so = WS_PP + (((team * 4 + sub) * 8 + 2 * w) * 8 + xcd) * 4608;
    const unsigned f4_so = team * FT + F4_OFF + (sub * 4 + w) * 256;
#define G4_MFMA(ac, mt, wf, s)                                                               \
    do {                                                                                     \
        if ((s) == 0) MFMA_VV0(ac, wf, own[mt][0]);                                          \
        else if ((s) < 4) MFMA_VV(ac, wf, own[mt][(s) < 4 ? (s) : 0]);                       \
        else if ((s) < 16) MFMA_VA(ac, wf, Hf[mt][(s) >= 4 && (s) < 16 ? (s)-4 : 0]);        \
        else MFMA_VV(ac, wf, Hc[mt][(s) >= 16 ? (s)-16 : 0]);                                \
    } while (0)
#define PART_STAGE(kk, P)                                                                    \
    do {                                                                                     \
        const int id_ = (kk) >> 1, mt_ = id_ >> 2, t_ = id_ & 3, e_ = ((kk)&1) * 2;          \
        po[(t_ & 1) * 2 + ((kk)&1)] = pk2(acc[mt_][4 * (P) + t_][e_], acc[mt_][4 * (P) + t_][e_ + 1]); \
        if (((kk)&3) == 3) ST4(po, vl16, sWS, pp_so + mt_ * 36864, (2 * (P) + (t_ >> 1)) * 1024); \
    } while (0)
    v4u po;
#pragma unroll
    for (int s = 0; s < 18; ++s) {
        if (s == 2) { WAITV(20); ST2(tagT, vone, sWS, f3_so + 32 + sub * 8, 0); }
        if (s >= 4 && s <= 10) WAITV_A2(s < 8 ? 23 : s == 8 ? 15 : s == 9 ? 14 : 11, Hf[0][s >= 4 && s <= 10 ? s - 4 : 0], Hf[1][s >= 4 && s <= 10 ? s - 4 : 0]);
        if (s == 8) LD2V(fl, vf4, sWS, f3_so, 32);
        if (s == 11) {
            POLL_CHECK(0, fl, vf4, sWS, f3_so + 32, tagT, dead);
#pragma unroll
            for (int u = 7; u < 12; ++u) asm volatile("" : "+a"(Hf[0][u]), "+a"(Hf[1][u]));
#pragma unroll
            for (int i = 0; i < 8; ++i) LD2V(hc[(i >> 1) & 1][i >> 2][i & 1], vl8, sWS, h_so + 16384 + ((i >> 1) & 1) * 147456, (i >> 2) * 1024 + (i & 1) * 512);
        }
        if (s >= 16) {
            const int u = s >= 16 ? s - 16 : 0;
            asm volatile("s_waitcnt vmcnt(%4)" : "+v"(hc[0][u][0]), "+v"(hc[0][u][1]), "+v"(hc[1][u][0]), "+v"(hc[1][u][1]) : "i"(s == 16 ? 4 : 0));
            Hc[0][u] = cat(hc[0][u][0], hc[0][u][1]); Hc[1][u] = cat(hc[1][u][0], hc[1][u][1]);
        }
#pragma unroll
        for (int t = 0; t < 4; ++t) {
            const v4u wf = cat(wl[t], wh[t]);
            G4_MFMA(acc[0][t], 0, wf, s);
            if (s < 17 && t > 0) wh[t - 1] = trd(lap[t - 1] + (s + 1) * 4096 + 2048);
            G4_MFMA(acc[1][t], 1, wf, s);
            if (s < 17) wl[t] = trd(lap[t] + (s + 1) * 4096);
            if (s < 17 && t == 3) wh[3] = trd(lap[3] + (s + 1) * 4096 + 2048);
            if (s == 17) { wl[t] = trd(lap[4 + t]); wh[t] = trd(lap[4 + t] + 2048); }
            if (s < 8 && t == 0) LOAD_H(0, s + 8);
            if (s < 8 && t == 2) LOAD_H(1, s + 8);
            SB();
        }
    }
    BARL();
    TS(12);
    glds(vpD, sW4, w4row + KPOS(14 + w) * 32 * 1152 + n4c, lw + LA);
#pragma unroll
    for (int s = 0; s < 18; ++s) {
        if (s == 10) { WAITV(0); ST2(tagT, vone, sWS, f4_so + xcd * 8, 0); }
#pragma unroll
        for (int t = 0; t < 4; ++t) {
            const v4u wf = cat(wl[t], wh[t]);
            G4_MFMA(acc[0][4 + t], 0, wf, s);
            if (s < 17 && t > 0) wh[t - 1] = trd(lap[3 + t] + (s + 1) * 4096 + 2048);
            if (s == 1 || s == 2) PART_STAGE((s - 1) * 8 + 2 * t, 0);
            G4_MFMA(acc[1][4 + t], 1, wf, s);
            if (s < 17) wl[t] = trd(lap[4 + t] + (s + 1) * 4096);
            if (s < 17 && t == 3) wh[3] = trd(lap[7] + (s + 1) * 4096 + 2048);
            if (s == 1 || s == 2) PART_STAGE((s - 1) * 8 + 2 * t + 1, 0);
            SB();
        }
    }
    BARL();
    TS(13);
    p2b = L0 + LA + vl8;
#pragma unroll
    for (int t = 0; t < 4; ++t) { wl[t] = P2RD(t, 0); wh[t] = P2RD(t, 512); }
#pragma unroll
    for (int kk = 0; kk < 16; ++kk) PART_STAGE(kk, 1);
    SB();
#pragma unroll
    for (int s = 0; s < 18; ++s) {
        const v4u wf = cat(wl[s & 3], wh[s & 3]);
        G4_MFMA(accC[0], 0, wf, s);
        G4_MFMA(accC[1], 1, wf, s);
        if (s + 4 < 18) { wl[s & 3] = P2RD(s + 4, 0); wh[s & 3] = P2RD(s + 4, 512); }
        SB();
    }
    NOP8();
    asm volatile("" : "+v"(accC[0]), "+v"(accC[1]));
#pragma unroll
    for (int mt = 0; mt < 2; ++mt) {
        v2u o; o.x = pk2(accC[mt][0], accC[mt][1]); o.y = pk2(accC[mt][2], accC[mt][3]);
        ST2(o, vl8, sWS, pp_so + mt * 36864 + 4096, 0);
    }
    TS(19);
    WAITV(2);
    ST2(tagT, vone, sWS, f4_so + 64 + xcd * 8, 0);
    WAITV(1);
    ST2(tagT, vone, sWS, f4_so + 128 + xcd * 8, 0);
    TS(14);
    {
        asm volatile("" : "+s"(w));
        const unsigned fr_so = team * FT + F4_OFF + (sub * 4 + (xcd >> 1)) * 256;
        const unsigned pr_so = WS_PP + (((team * 4 + sub) * 8 + xcd) * 8) * 4608;
        const int tileo = team * 8 + xcd;
        const unsigned vo = m * 1152 + g * 8;
        poll((lane & 7) * 8 + ((w == 0 && (lane & 8)) ? 128 : (w >> 1) * 64), sWS, fr_so, tagT, dead);
        v4u p[8], xr;
        v2u c[8], xc;
#pragma unroll
        for (int i = 0; i < 8; ++i) LD4V(p[i], vl16, sWS, pr_so + i * 4608 + w * 1024, 0);
        LD4V(xr, vl16, sWS, WS_X1F + (tileo * 18 + 4 * sub + w) * 1024, 0);
        const unsigned vc8 = w == 0 ? vl8 : OOB, vc16 = w == 0 ? vl16 : OOB, voc = w == 0 ? vo : OOB;
#pragma unroll
        for (int i = 0; i < 8; ++i) LD2V(c[i], vc8, sWS, pr_so + i * 4608 + 4096, 0);
        LD2V(xc, vc16, sWS, WS_X1F + (tileo * 18 + 16 + (sub >> 1)) * 1024 + (sub & 1) * 8, 0);
        asm volatile("s_waitcnt vmcnt(9)" : "+v"(p[0]), "+v"(p[1]), "+v"(p[2]), "+v"(p[3]), "+v"(p[4]), "+v"(p[5]), "+v"(p[6]), "+v"(p[7]), "+v"(xr));
        v4u o;
#pragma unroll
        for (int e = 0; e < 4; ++e) {
            float a0 = 0.f, a1 = 0.f;
#pragma unroll
            for (int i = 0; i < 8; ++i) { a0 += blo(p[i][e]); a1 += bhi(p[i][e]); }
            const unsigned hv = pk2(a0, a1);
            o[e] = pk2(blo(xr[e]) + blo(hv), bhi(xr[e]) + bhi(hv));
        }
        asm volatile("s_waitcnt vmcnt(0)" : "+v"(c[0]), "+v"(c[1]), "+v"(c[2]), "+v"(c[3]), "+v"(c[4]), "+v"(c[5]), "+v"(c[6]), "+v"(c[7]), "+v"(xc));
        v2u o0 = {o.x, o.y}, o1 = {o.z, o.w};
        ST2P(o0, vo, sO, tileo * 16 * 1152 + (128 * sub + 32 * w) * 2, 0);
        ST2P(o1, vo, sO, tileo * 16 * 1152 + (128 * sub + 32 * w) * 2, 32);
        v2u oc;
#pragma unroll
        for (int e = 0; e < 2; ++e) {
            float a0 = 0.f, a1 = 0.f;
#pragma unroll
            for (int i = 0; i < 8; ++i) { a0 += blo(c[i][e]); a1 += bhi(c[i][e]); }
            const unsigned hv = pk2(a0, a1);
            oc[e] = pk2(blo(xc[e]) + blo(hv), bhi(xc[e]) + bhi(hv));
        }
        ST2P(oc, voc, sO, tileo * 16 * 1152 + (512 + 16 * sub) * 2, 0);
        if (xcd == 0 && sub == 0 && w == 0) ST2(tagT, vone, sWS, team * FT, 0);
    }
    asm volatile(".Lmx_end:");
}

extern "C" void kernel_launch(void* const* d_in, const int* in_sizes, int n_in, void* d_out, int out_size, void* d_ws, size_t ws_size,
                              hipStream_t stream) {
    mixer_kernel<<<256, 256, 0, stream>>>((const u16*)d_in[0], (const u16*)d_in[1], (const u16*)d_in[2], (const u16*)d_in[3],
                                          (const u16*)d_in[4], (u16*)d_out, (unsigned char*)d_ws);
}
```

```cpp
#define PADFROM 0
#include <hip/hip_runtime.h>
#include <stdint.h>

typedef unsigned short u16;
typedef unsigned v4u __attribute__((ext_vector_type(4)));
typedef unsigned v2u __attribute__((ext_vector_type(2)));
typedef short s16x4 __attribute__((ext_vector_type(4)));
typedef short bf16x8 __attribute__((ext_vector_type(8)));
typedef float f32x4 __attribute__((ext_vector_type(4)));
typedef float f32x2 __attribute__((ext_vector_type(2)));
typedef __bf16 bf16x2 __attribute__((ext_vector_type(2)));
#define LAS __attribute__((address_space(3)))
#define DI __device__ __forceinline__

#define WS_FLAG 0
#define WS_HF 262144
#define WS_X1F (WS_HF + 1179648)
#define WS_ST (WS_X1F + 1179648)
#define WS_HH (WS_ST + 73728)
#define WS_PP (WS_HH + 9437184)
#define WS_DBG (WS_PP + 9437184)
#define WS_TOTAL (WS_DBG + 131072)
constexpr int FT = 0x8000, F1_OFF = 0x100, F2_OFF = 0x800, F3_OFF = 0x2800, F4_OFF = 0x3800;
constexpr int LA = 0, LB = 73728, LC = 147456, LM = 161792;
constexpr int SPIN_CAP = 40000;
#define WARM_SLEEP 24

DI v4u mk_srd(const void* p, unsigned bytes) {
    unsigned long long a = (unsigned long long)p;
    v4u s; s.x = (unsigned)a; s.y = (unsigned)(a >> 32) & 0xffffu; s.z = bytes; s.w = 0x20000u; return s;
}
DI void glds(unsigned voff, v4u srd, unsigned soff, unsigned lds) {
    asm volatile("s_mov_b32 m0, %3\n\ts_nop 0\n\tbuffer_load_dwordx4 %0, %1, %2 offen lds" ::"v"(voff), "s"(srd), "s"(soff), "s"(lds));
}
#define LD4A(d, voff, srd, soff, imm) asm volatile("buffer_load_dwordx4 %0, %1, %2, %3 offen offset:%4 sc1" : "=a"(d) : "v"(voff), "s"(srd), "s"(soff), "i"(imm))
#define LD4V(d, voff, srd, soff, imm) asm volatile("buffer_load_dwordx4 %0, %1, %2, %3 offen offset:%4 sc1" : "=v"(d) : "v"(voff), "s"(srd), "s"(soff), "i"(imm))
#define LD2V(d, voff, srd, soff, imm) asm volatile("buffer_load_dwordx2 %0, %1, %2, %3 offen offset:%4 sc1" : "=v"(d) : "v"(voff), "s"(srd), "s"(soff), "i"(imm))
#define LD2A(d, voff, srd, soff, imm) asm volatile("buffer_load_dwordx2 %0, %1, %2, %3 offen offset:%4 sc1" : "=a"(d) : "v"(voff), "s"(srd), "s"(soff), "i"(imm))
#define LD4W(d, voff, srd, soff) asm volatile("buffer_load_dwordx4 %0, %1, %2, %3 offen" : "+v"(d) : "v"(voff), "s"(srd), "s"(soff))
#define ST4(d, voff, srd, soff, imm) asm volatile("buffer_store_dwordx4 %0, %1, %2, %3 offen offset:%4 sc1\n\ts_nop 1" ::"v"(d), "v"(voff), "s"(srd), "s"(soff), "i"(imm))
#define ST2(d, voff, srd, soff, imm) asm volatile("buffer_store_dwordx2 %0, %1, %2, %3 offen offset:%4 sc1" ::"v"(d), "v"(voff), "s"(srd), "s"(soff), "i"(imm))
#define ST2P(d, voff, srd, soff, imm) asm volatile("buffer_store_dwordx2 %0, %1, %2, %3 offen offset:%4" ::"v"(d), "v"(voff), "s"(srd), "s"(soff), "i"(imm))
#define WAITV(n) asm volatile("s_waitcnt vmcnt(%0)" ::"i"(n) : "memory")
#define WAITV_A2(n, a, b) asm volatile("s_waitcnt vmcnt(%2)" : "+a"(a), "+a"(b) : "i"(n))
#define WAITV_V2(n, a, b) asm volatile("s_waitcnt vmcnt(%2)" : "+v"(a), "+v"(b) : "i"(n))
#define WAITV_V1(n, a) asm volatile("s_waitcnt vmcnt(%1)" : "+v"(a) : "i"(n))
#define BARV(n) asm volatile("s_waitcnt vmcnt(%0) lgkmcnt(0)\n\ts_barrier" ::"i"(n) : "memory")
#define BARL() asm volatile("s_waitcnt lgkmcnt(0)\n\ts_barrier" ::: "memory")
#define SB() __builtin_amdgcn_sched_barrier(0)
#define MFMA_VA(c, a, b) asm volatile("v_mfma_f32_16x16x32_bf16 %0, %1, %2, %0" : "+v"(c) : "v"(a), "a"(b))
#define MFMA_VV(c, a, b) asm volatile("v_mfma_f32_16x16x32_bf16 %0, %1, %2, %0" : "+v"(c) : "v"(a), "v"(b))
#define MFMA_VV0(c, a, b) asm volatile("v_mfma_f32_16x16x32_bf16 %0, %1, %2, 0" : "=&v"(c) : "v"(a), "v"(b))
#define MFMA_VA0(c, a, b) asm volatile("v_mfma_f32_16x16x32_bf16 %0, %1, %2, 0" : "=&v"(c) : "v"(a), "a"(b))
#define GL_INC(voff, srd, so, lw, limm, sinc) asm volatile("s_add_u32 m0, %3, %4\n\ts_add_u32 %0, %0, %5\n\tbuffer_load_dwordx4 %1, %2, %0 offen lds" : "+s"(so) : "v"(voff), "s"(srd), "s"(lw), "i"(limm), "i"(sinc) : "scc")
#define GL_Q(voff, srd, so, lw, limm, off) asm volatile("s_add_u32 m0, %3, %4\n\ts_nop 0\n\tbuffer_load_dwordx4 %0, %1, %2 offen offset:%5 lds" ::"v"(voff), "s"(srd), "s"(so), "s"(lw), "i"((limm) - (off)), "i"(off) : "scc")
#define OPQ(x) asm volatile("" : "+v"(x))
#define NOP8() asm volatile("s_nop 7\n\ts_nop 1" ::: "memory")

DI v2u trd(unsigned addr) { return __builtin_bit_cast(v2u, __builtin_amdgcn_ds_read_tr16_b64_v4i16((LAS s16x4*)addr)); }
DI v4u cat(v2u a, v2u b) { v4u r; r.x = a.x; r.y = a.y; r.z = b.x; r.w = b.y; return r; }
DI f32x4 mfma_b(v4u a, v4u b, f32x4 c) {
    return __builtin_amdgcn_mfma_f32_16x16x32_bf16(__builtin_bit_cast(bf16x8, a), __builtin_bit_cast(bf16x8, b), c, 0, 0, 0);
}
DI unsigned pk2(float a, float b) { bf16x2 p; p.x = (__bf16)a; p.y = (__bf16)b; return __builtin_bit_cast(unsigned, p); }
DI float blo(unsigned u) { return __uint_as_float(u << 16); }
DI float bhi(unsigned u) { return __uint_as_float(u & 0xffff0000u); }
DI float xsum4(float x) {
    auto s = __builtin_amdgcn_permlane16_swap(__float_as_uint(x), __float_as_uint(x), false, false);
    x = __uint_as_float(s[0]) + __uint_as_float(s[1]);
    auto t = __builtin_amdgcn_permlane32_swap(__float_as_uint(x), __float_as_uint(x), false, false);
    return __uint_as_float(t[0]) + __uint_as_float(t[1]);
}
constexpr float GK1 = -2.3022081986f, GK2 = -0.1029432397f;
DI float gelu1(float z) {
    float e = __builtin_amdgcn_exp2f(z * __builtin_fmaf(z * z, GK2, GK1));
    return z * __builtin_amdgcn_rcpf(1.0f + e);
}
DI void poll(unsigned voff, v4u srd, unsigned soff, v2u tag, int& dead) {
    if (dead) return;
    for (int it = 0;; ++it) {
        v2u f;
        asm volatile("buffer_load_dwordx2 %0, %1, %2, %3 offen sc1\n\ts_waitcnt vmcnt(0)" : "=&v"(f) : "v"(voff), "s"(srd), "s"(soff) : "memory");
        if (__all(f.x == tag.x && f.y == tag.y)) return;
        if (it > SPIN_CAP) { dead = 1; return; }
        __builtin_amdgcn_s_sleep(1);
    }
}
#define POLL_CHECK(n, f, voff, srd, soff, tag, dead) \
    do { WAITV_V1(n, f); if (!__all(f.x == tag.x && f.y == tag.y)) poll(voff, srd, soff, tag, dead); } while (0)

#define TS(i) do { if ((i) == PADFROM || (i) == 0) asm volatile("s_memrealtime %0\n\ts_waitcnt lgkmcnt(0)" : "=s"(tpad)::"memory"); } while (0)

DI unsigned gelu_pair(f32x2 v, float c1, float c2, float ia) {
    const f32x2 t = v * __builtin_elementwise_fma(v * v, f32x2{c2, c2}, f32x2{c1, c1});
    f32x2 e; e.x = __builtin_amdgcn_exp2f(t.x); e.y = __builtin_amdgcn_exp2f(t.y);
    const f32x2 d = __builtin_elementwise_fma(e, f32x2{ia, ia}, f32x2{ia, ia});
    f32x2 r; r.x = __builtin_amdgcn_rcpf(d.x); r.y = __builtin_amdgcn_rcpf(d.y);
    const f32x2 y = v * r;
    return pk2(y.x, y.y);
}
DI v2u gelu_acc(f32x4 a, f32x4 cs, float nm, float c1, float c2, float ia) {
    const f32x4 v = __builtin_elementwise_fma(cs, f32x4{nm, nm, nm, nm}, a);
    v2u o; o.x = gelu_pair(f32x2{v[0], v[1]}, c1, c2, ia); o.y = gelu_pair(f32x2{v[2], v[3]}, c1, c2, ia);
    return o;
}

__global__ __launch_bounds__(256, 1) void mixer_kernel(const u16* __restrict__ x, const u16* __restrict__ w1, const u16* __restrict__ w2,
                                                       const u16* __restrict__ w3, const u16* __restrict__ w4, u16* __restrict__ out,
                                                       unsigned char* __restrict__ ws) {
    __shared__ __attribute__((aligned(1024))) unsigned char smem[163840];
    unsigned long long tpad;
    TS(0);
    const int b = blockIdx.x, xcd = b & 7, j = b >> 3, team = j & 7, sub = j >> 3;
    const int lane = threadIdx.x & 63;
    int w = __builtin_amdgcn_readfirstlane(threadIdx.x >> 6);
    const int m = lane & 15, g = lane >> 4, q = m >> 2, r8 = lane >> 3, pos = lane & 7;
    const unsigned L0 = (unsigned)(size_t)(LAS unsigned char*)smem, lw = L0 + w * 1024;
    const v4u sX = mk_srd(x, 1179648), sW1 = mk_srd(w1, 663552), sW2 = mk_srd(w2, 663552), sW3 = mk_srd(w3, 5308416),
              sW4 = mk_srd(w4, 5308416), sO = mk_srd(out, 1179648), sWS = mk_srd(ws, WS_TOTAL);
    const unsigned vl16 = lane * 16, vl8 = lane * 8;
    constexpr unsigned OOB = 0x80000000u;
    const unsigned vone = lane == 0 ? 0u : OOB;
    const unsigned swzc = (((pos >> 1) ^ ((r8 >> 1) & 3)) << 5) + (pos & 1) * 16;
    const unsigned vwD = r8 * 1152 + swzc, vwF = r8 * 9216 + swzc;
    const unsigned vpD = (lane >> 1) * 1152 + (lane & 1) * 16, vpF = (lane >> 1) * 9216 + (lane & 1) * 16;
    const unsigned n3 = (576 * xcd + 128 * sub) * 2, n3c = (576 * xcd + 512 + 16 * sub) * 2;
    const bool act = j < 18;
    v4u junk = {0u, 0u, 0u, 0u};
    {
        unsigned long long pc; unsigned len;
        asm volatile("s_getpc_b64 %0\n.Lmx_here:\n\ts_mov_b32 %1, .Lmx_end-.Lmx_here" : "=s"(pc), "=s"(len));
        v4u sC; sC.x = (unsigned)pc; sC.y = (unsigned)(pc >> 32) & 0xffffu; sC.z = len; sC.w = 0x20000u;
        LD4W(junk, vl16, sC, (j * 4 + w) * 1024);
    }
    {
        const int wv = j * 4 + w;
#pragma unroll
        for (int i = 0; i < 5; ++i) LD4W(junk, vl16, sW1, ((wv + 128 * i + 81 * xcd) % 648) * 1024);
        LD4W(junk, vl16, sW1, wv < 8 ? ((wv + 640 + 81 * xcd) % 648) * 1024 : OOB);
        LD4W(junk, vl16, sX, xcd * 147456 + wv * 1024);
        LD4W(junk, vl16, sX, wv < 16 ? xcd * 147456 + (128 + wv) * 1024 : OOB);
    }
    int dead = 0;
    v2u tagR, tagT;
    unsigned lap[8];
    {
        const int r4 = 4 * (g & 1) + q, xp = (r4 >> 1) & 3;
#pragma unroll
        for (int t = 0; t < 4; ++t) {
            lap[t] = L0 + LA + (g >> 1) * 1024 + r4 * 128 + ((t ^ xp) << 5) + (lane & 3) * 8;
            lap[4 + t] = lap[t] + (LB - LA);
            OPQ(lap[4 + t]);
        }
    }
    v4u ones = {0x3f803f80u, 0x3f803f80u, 0x3f803f80u, 0x3f803f80u};
    OPQ(ones);
#define LOAD_EPOCHS() do { LD2V(tagR, 0u, sWS, xcd * FT, 0); LD2V(tagT, 0u, sWS, team * FT, 0); } while (0)
#define TAKE_EPOCHS(n) do { WAITV_V2(n, tagR, tagT); tagR.x += 1; tagT.x += 1; } while (0)
#define HEAD_PIECE(i) ((w + 4 * (i) < 14) ? w + 4 * (i) : w + 8)

    if (act) {
        const int rh = (j * 29) >> 8, c9 = j - 9 * rh, tok0 = xcd * 128 + rh * 64;
        const unsigned vx0 = r8 * 1152 + ((pos ^ (r8 >> 1)) << 4), vx1 = r8 * 1152 + ((pos ^ ((r8 >> 1) | 4)) << 4);
        const unsigned vw1 = r8 * 1152 + (((pos >> 1) ^ (((r8 >> 1) & 1) | ((w & 1) << 1))) << 5) + (pos & 1) * 16;
#define G1_LINE(u) (c9 + (u) >= 9 ? c9 + (u)-9 : c9 + (u))
#define G1_X(u) do { glds(vx0, sX, (tok0 + 16 * w) * 1152 + G1_LINE(u) * 128, L0 + LB + (2 * w * 9 + (u)) * 1024); \
                     glds(vx1, sX, (tok0 + 16 * w + 8) * 1152 + G1_LINE(u) * 128, L0 + LB + ((2 * w + 1) * 9 + (u)) * 1024); } while (0)
#define G1_W(u) do { glds(vw1, sW1, (G1_LINE(u) * 64 + 8 * w) * 1152 + c9 * 128, lw + LA + (u)*8192); \
                     glds(vw1, sW1, (G1_LINE(u) * 64 + 8 * w + 32) * 1152 + c9 * 128, lw + LA + (u)*8192 + 4096); } while (0)
        LOAD_EPOCHS();
#pragma unroll
        for (int u = 0; u < 6; ++u) { G1_X(u); G1_W(u); }
        TAKE_EPOCHS(24);
        const int swz = (m >> 1) & 7;
        const unsigned xrow = L0 + LB + (2 * w + (m >> 3)) * 9216 + (m & 7) * 128;
        unsigned xa0 = xrow + ((g ^ swz) << 4), xa1 = xrow + ((g ^ swz ^ 4) << 4);
        OPQ(xa0); OPQ(xa1);
        unsigned la1[4];
        {
            const int x1 = (q >> 1) | ((g & 1) << 1);
#pragma unroll
            for (int t = 0; t < 4; ++t) la1[t] = L0 + LA + g * 1024 + q * 128 + ((t ^ w ^ x1) << 5) + (lane & 3) * 8;
        }
        f32x4 acc[4], acs, asum;
        float ssq = 0.f;
        v2u xres[4], wl[2][4], wh[2][4];
        v4u xf[2];
#define G1_RDX(s) xf[(s)&1] = *(LAS v4u*)(((s)&1 ? xa1 : xa0) + ((s) >> 1) * 1024)
#define G1_RDL(s, t) wl[(s)&1][t] = trd(la1[t] + (s)*4096)
#define G1_RDH(s, t) wh[(s)&1][t] = trd(la1[t] + (s)*4096 + 512)
#pragma unroll
        for (int s = 0; s < 18; ++s) {
            if (s == 0) { BARV(12); OPQ(junk); TS(1); }
            if (s == 6) BARV(12);
            if (s == 12) BARV(4);
            if (s == 0 || s == 6 || s == 12) {
                G1_RDX(s);
#pragma unroll
                for (int t = 0; t < 4; ++t) { G1_RDL(s, t); G1_RDH(s, t); }
            }
            if (s == 0) {
#pragma unroll
                for (int t = 0; t < 4; ++t) xres[t] = *(LAS v2u*)(xrow + (((2 * t + (g >> 1)) ^ swz) << 4) + (g & 1) * 8);
            }
            if (s < 6) { if (s & 1) G1_W(6 + (s >> 1)); else G1_X(6 + (s >> 1)); }
            else if (s < 10) glds(vpF, sW3, HEAD_PIECE(s - 6) * 32 * 9216 + n3c, L0 + LC + HEAD_PIECE(s - 6) * 1024);
            const bool nx = s != 5 && s != 11 && s != 17;
            const v4u xc = xf[s & 1];
            if (nx) G1_RDX(s + 1);
#pragma unroll
            for (int t = 0; t < 4; ++t) {
                const v4u wf = cat(wl[s & 1][t], wh[s & 1][t]);
                if (s == 0) MFMA_VV0(acc[t], wf, xc); else MFMA_VV(acc[t], wf, xc);
                if (nx) G1_RDL(s + 1, t);
                if (t == 0) { if (s == 0) MFMA_VV0(acs, wf, ones); else MFMA_VV(acs, wf, ones); }
                if (t == 1) { if (s == 0) MFMA_VV0(asum, ones, xc); else MFMA_VV(asum, ones, xc); }
                if (nx) G1_RDH(s + 1, t);
                const unsigned xe = xc[t];
                ssq = __builtin_amdgcn_fdot2_f32_bf16(__builtin_bit_cast(bf16x2, xe), __builtin_bit_cast(bf16x2, xe), ssq, false);
                SB();
            }
        }
        NOP8();
        asm volatile("" : "+v"(acs), "+v"(asum), "+v"(acc[0]), "+v"(acc[1]), "+v"(acc[2]), "+v"(acc[3]));
        if (m == 0) *(LAS f32x4*)(L0 + LM + w * 64 + g * 16) = acs;
        BARL();
        TS(2);
        const unsigned fb = xcd * FT + (rh * 2 + (w >> 1)) * 256, fi = (c9 * 2 + (w & 1)) * 8;
        {
            const float mean = asum[0] * (1.0f / 576.0f);
            const float var = __builtin_fmaxf(__builtin_fmaf(-mean, mean, xsum4(ssq) * (1.0f / 576.0f)), 0.f) + 1e-5f;
            const float a = __builtin_amdgcn_rsqf(var);
            const int tok = 16 * (w & 1) + m, batch = xcd * 4 + rh * 2 + (w >> 1);
            v2u o[4];
#pragma unroll
            for (int t = 0; t < 4; ++t) o[t] = gelu_acc(acc[t], *(LAS f32x4*)(L0 + LM + (t ^ w) * 64 + g * 16), -mean, a * GK1, a * a * a * GK2, var * a);
#pragma unroll
            for (int pp = 0; pp < 2; ++pp) {
                const v2u lo = (w & 1) ? o[2 * pp + 1] : o[2 * pp], hi = (w & 1) ? o[2 * pp] : o[2 * pp + 1];
                const int Te = 4 * c9 + 2 * ((w >> 1) ^ pp);
                const unsigned so = WS_HF + (batch * 36 + (Te >> 1)) * 1024;
                const int sh = Te < 9 ? 1 : Te < 18 ? -1 : Te < 27 ? 2 : -2, dest = tok + sh;
                const bool in = dest >= 0 && dest < 32;
                if (Te == 8 || Te == 26) {
                    const int d2 = tok - sh;
                    const bool in2 = d2 >= 0 && d2 < 32;
                    ST2((in ? lo : v2u{0u, 0u}), ((dest >> 4) & 1) * 18432 + g * 128 + (dest & 15) * 8, sWS, so, 0);
                    ST2((in2 ? hi : v2u{0u, 0u}), ((d2 >> 4) & 1) * 18432 + g * 128 + (d2 & 15) * 8, sWS, so, 512);
                } else {
                    ST4((in ? cat(lo, hi) : v4u{0u, 0u, 0u, 0u}), ((dest >> 4) & 1) * 18432 + g * 256 + (dest & 15) * 16, sWS, so, 0);
                }
            }
            TS(16);
            WAITV(0);
            ST2(tagR, vone, sWS, fb + F1_OFF + fi, 0);
        }
        TS(3);
        {
            unsigned so = (8 * w - 32) * 1152 + c9 * 128;
#pragma unroll
            for (int i = 0; i < 18; ++i) GL_INC(vwD, sW2, so, lw, LB + i * 4096, 32 * 1152);
        }
        poll((lane < 18 ? lane : 0) * 8, sWS, fb + F1_OFF, tagR, dead);
        TS(4);
        const int tile = xcd * 8 + rh * 4 + w;
        v4u hf[18];
        v2u hs[4];
#pragma unroll
        for (int s = 0; s < 18; ++s) {
            const unsigned so = WS_HF + (tile * 18 + (s & ~3)) * 1024;
            if (s == 4 || s == 13) { LD2V(hs[s == 4 ? 0 : 2], vl8, sWS, so, (s & 3) * 1024); LD2V(hs[s == 4 ? 1 : 3], vl8, sWS, so, (s & 3) * 1024 + 512); }
            else LD4V(hf[s], vl16, sWS, so, (s & 3) * 1024);
        }
        BARL();
#pragma unroll
        for (int t = 0; t < 4; ++t) { wl[0][t] = trd(lap[4 + t]); wh[0][t] = trd(lap[4 + t] + 2048); }
        unsigned so3 = (8 * w - 32) * 9216 + n3;
#pragma unroll
        for (int s = 0; s < 18; ++s) {
            if (s == 4 || s == 13) { WAITV_V2(17 + (s < 13), hs[s == 4 ? 0 : 2], hs[s == 4 ? 1 : 3]); hf[s] = cat(hs[s == 4 ? 0 : 2], hs[s == 4 ? 1 : 3]); }
            else WAITV_V1(17 + (s < 4) + (s < 13), hf[s]);
            GL_INC(vwF, sW3, so3, lw, LA + s * 4096, 32 * 9216);
#pragma unroll
            for (int t = 0; t < 4; ++t) {
                const v4u wf = cat(wl[s & 1][t], wh[s & 1][t]);
                if (s == 0) MFMA_VV0(acc[t], wf, hf[s]); else MFMA_VV(acc[t], wf, hf[s]);
                if (s < 17) { wl[(s + 1) & 1][t] = trd(lap[4 + t] + (s + 1) * 4096); wh[(s + 1) & 1][t] = trd(lap[4 + t] + (s + 1) * 4096 + 2048); }
                SB();
            }
        }
        NOP8();
        asm volatile("" : "+v"(acc[0]), "+v"(acc[1]), "+v"(acc[2]), "+v"(acc[3]));
        TS(5);
        {
            float s1 = 0.f, s2 = 0.f;
            v4u o[2];
#pragma unroll
            for (int t = 0; t < 4; ++t) {
#pragma unroll
                for (int h2 = 0; h2 < 2; ++h2) {
                    const unsigned hv = pk2(acc[t][2 * h2], acc[t][2 * h2 + 1]);
                    const unsigned xv = xres[t][h2];
                    const unsigned y = pk2(blo(xv) + blo(hv), bhi(xv) + bhi(hv));
                    o[t >> 1][(t & 1) * 2 + h2] = y;
                    const float a0 = blo(y), a1 = bhi(y);
                    s1 += a0 + a1;
                    s2 = __builtin_fmaf(a0, a0, __builtin_fmaf(a1, a1, s2));
                }
            }
            ST4(o[0], vl16, sWS, WS_X1F + (tile * 18 + 2 * c9) * 1024, 0);
            ST4(o[1], vl16, sWS, WS_X1F + (tile * 18 + 2 * c9) * 1024, 1024);
            v2u st; st.x = __float_as_uint(xsum4(s1)); st.y = __float_as_uint(xsum4(s2));
            ST2(st, g == 0 ? vl8 : OOB, sWS, WS_ST + (tile * 9 + c9) * 128, 0);
            WAITV(0);
            ST2(tagR, lane < 8 ? lane * 1024 : OOB, sWS, fb + F2_OFF + fi, 0);
        }
        TS(6);
        BARL();
    } else {
        LOAD_EPOCHS();
        {
            unsigned d1 = 0;
            if (j < 20) {
#pragma unroll
                for (int i = 0; i < 1; ++i) asm volatile("buffer_load_dword %0, %1, %2, %3 offen" : "+v"(d1) : "v"(lane * 65536u), "s"(sWS), "s"((((j - 18) * 4 + w) * 64) * 65536u));
            } else if (j == 20 && w == 0) asm volatile("buffer_load_dword %0, %1, %2, 0 offen" : "+v"(d1) : "v"(lane * 65536u), "s"(sO));
            WAITV(0);
            OPQ(d1);
        }
        TAKE_EPOCHS(0);
        for (int i = 0; i < WARM_SLEEP; ++i) __builtin_amdgcn_s_sleep(2);
        const int wv = (j - 18) * 4 + w;
        v4u dummy = junk;
#pragma unroll
        for (int i = 0; i < 12; ++i) { const int p = wv + 56 * i; LD4W(dummy, vl16, sW2, (p < 648 ? p : wv) * 1024); }
#pragma unroll
        for (int i = 0; i < 11; ++i) { const int p = wv + 56 * i; LD4W(dummy, vl16, sW3, (p < 576 ? p : wv) * 9216 + 576 * 2 * xcd); }
#pragma unroll
        for (int i = 0; i < 2; ++i) { const int p = wv + 56 * i; LD4W(dummy, r8 * 9216 + pos * 16, sW3, (p < 72 ? p : wv) * 8 * 9216 + (576 * xcd + 512) * 2); }
        unsigned so3 = (8 * w - 32) * 9216 + n3;
#pragma unroll
        for (int i = 0; i < 18; ++i) GL_INC(vwF, sW3, so3, lw, LA + i * 4096, 32 * 9216);
#pragma unroll
        for (int i = 0; i < 4; ++i) glds(vpF, sW3, HEAD_PIECE(i) * 32 * 9216 + n3c, L0 + LC + HEAD_PIECE(i) * 1024);
#pragma unroll
        for (int i = 0; i < 12; ++i) { const int p = wv + 56 * i; LD4W(dummy, vl16, sW4, 576 * 1152 * xcd + (p < 648 ? p : wv) * 1024); }
        TS(5);
        BARV(0);
        OPQ(dummy);
        TS(6);
    }

    asm volatile("" : "+s"(w));
    {
        unsigned lapw = L0 + LA + (g >> 1) * 1024 + (4 * (g & 1) + q) * 128 + ((w ^ ((2 * (g & 1) + (q >> 1)) & 3)) << 5) + (lane & 3) * 8;
        unsigned so3 = (8 * w - 32) * 9216 + n3 + 128;
#pragma unroll
        for (int P = 0; P < 2; ++P) {
            f32x4 cs;
            v2u cl[18], ch[18];
#pragma unroll
            for (int s = 0; s < 4; ++s) { cl[s] = trd(lapw + s * 4096); ch[s] = trd(lapw + s * 4096 + 2048); }
#pragma unroll
            for (int s = 0; s < 18; ++s) {
                if (P == 0) GL_INC(vwF, sW3, so3, lw, LB + s * 4096, 32 * 9216);
                if (s == 0) MFMA_VV0(cs, cat(cl[s], ch[s]), ones); else MFMA_VV(cs, cat(cl[s], ch[s]), ones);
                if (s + 4 < 18) { cl[s + 4] = trd(lapw + (s + 4) * 4096); ch[s + 4] = trd(lapw + (s + 4) * 4096 + 2048); }
                SB();
            }
            if (P == 0) { BARV(0); lapw += LB - LA; OPQ(lapw); }
            NOP8();
            OPQ(cs);
            if (m == 0) *(LAS f32x4*)(L0 + LM + 256 + P * 256 + w * 64 + g * 16) = cs;
        }
    }
    TS(17);
    poll((lane < 18 ? lane : 0) * 8, sWS, team * FT + F2_OFF + xcd * 1024 + w * 256, tagT, dead);
    TS(7);
    const int tile0 = team * 8 + 2 * w;
    v2u stp[2][3];
    {
        const unsigned vs = g * 128 + m * 8, vs2 = g == 0 ? 1024 + m * 8 : OOB;
#pragma unroll
        for (int mt = 0; mt < 2; ++mt) {
            LD2V(stp[mt][0], vs, sWS, WS_ST + (tile0 + mt) * 1152, 0);
            LD2V(stp[mt][1], vs, sWS, WS_ST + (tile0 + mt) * 1152, 512);
            LD2V(stp[mt][2], vs2, sWS, WS_ST + (tile0 + mt) * 1152, 0);
        }
    }
    v4u X[2][18];
    const unsigned x1_so = WS_X1F + tile0 * 18432;
#define LOAD_X(mt, s) LD4A(X[mt][s], vl16, sWS, x1_so + (mt)*18432 + ((s) >> 2) * 4096, ((s)&3) * 1024)
    constexpr int PF = 5;
#pragma unroll
    for (int s = 0; s < PF; ++s) { LOAD_X(0, s); LOAD_X(1, s); }
    v2u wl[8], wh[8];
#pragma unroll
    for (int t = 0; t < 8; ++t) { wl[t] = trd(lap[t]); wh[t] = trd(lap[t] + 2048); }
    float nmean[2], c1[2], c2[2], ia[2];
    asm volatile("s_waitcnt vmcnt(%6)" : "+v"(stp[0][0]), "+v"(stp[0][1]), "+v"(stp[0][2]), "+v"(stp[1][0]), "+v"(stp[1][1]), "+v"(stp[1][2]) : "i"(2 * PF));
#pragma unroll
    for (int mt = 0; mt < 2; ++mt) {
        const float s1 = xsum4(__uint_as_float(stp[mt][0].x) + __uint_as_float(stp[mt][1].x) + __uint_as_float(stp[mt][2].x));
        const float s2 = xsum4(__uint_as_float(stp[mt][0].y) + __uint_as_float(stp[mt][1].y) + __uint_as_float(stp[mt][2].y));
        const float mean = s1 * (1.0f / 576.0f);
        const float var = __builtin_fmaxf(__builtin_fmaf(-mean, mean, s2 * (1.0f / 576.0f)), 0.f) + 1e-5f;
        const float a = __builtin_amdgcn_rsqf(var);
        nmean[mt] = -mean; c1[mt] = a * GK1; c2[mt] = a * a * a * GK2; ia[mt] = var * a;
        asm volatile("" : "+v"(nmean[mt]), "+v"(c1[mt]), "+v"(c2[mt]), "+v"(ia[mt]));
    }
    f32x4 acc[2][8], accC[2], csC;
    SB();
    TS(8);
#pragma unroll
    for (int s = 0; s < 18; ++s) {
        WAITV_A2(s + PF <= 18 ? 2 * (PF - 1) : 2 * (17 - s), X[0][s], X[1][s]);
#pragma unroll
        for (int t = 0; t < 8; ++t) {
            const v4u wf = cat(wl[t], wh[t]);
            if (s == 0) MFMA_VA0(acc[0][t], wf, X[0][s]); else MFMA_VA(acc[0][t], wf, X[0][s]);
            if (s < 17 && t > 0) wh[t - 1] = trd(lap[t - 1] + (s + 1) * 4096 + 2048);
            if (s > 0 && t == 0) {}
            if (s == 0) MFMA_VA0(acc[1][t], wf, X[1][s]); else MFMA_VA(acc[1][t], wf, X[1][s]);
            if (s < 17) wl[t] = trd(lap[t] + (s + 1) * 4096);
            if (s < 17 && t == 7) wh[7] = trd(lap[7] + (s + 1) * 4096 + 2048);
            if (s + PF < 18 && t == 1) LOAD_X(0, s + PF);
            if (s + PF < 18 && t == 5) LOAD_X(1, s + PF);
            SB();
        }
    }
    BARL();
    TS(9);
    glds(vpF, sW3, (14 + w) * 32 * 9216 + n3c, lw + LA + 17 * 4096);
#define P2ADDR(s) (L0 + vl8 + ((s) < 14 ? LC + (s)*1024 : LA + 17 * 4096 + ((s)-14) * 1024))
    unsigned p2a = L0 + LC + vl8, p2b = L0 + LA + 17 * 4096 + vl8;
    OPQ(p2a); OPQ(p2b);
    const unsigned w4row = 576 * 1152 * xcd, n4c = (512 + 16 * sub) * 2;
    unsigned sb[5], vw4[4];
#pragma unroll
    for (int i = 0; i < 4; ++i) { sb[i] = w4row + (((sub + i) & 3) * 128 + 8 * w) * 1152 + 256 * sub; vw4[i] = vwD + i * 32 * 1152; }
    sb[4] = w4row + (512 + 8 * w) * 1152 + 256 * sub;
#define KPOS(u) ((u) < 16 ? ((4 * sub + (u)) & 15) : (u))
#define LOAD_Q(P, u) GL_Q(vw4[(u)&3], sW4, sb[(u) >> 2], lw, ((P) ? LB : LA) + (u)*4096, (P)*128)
    v4u own[2][4];
    const unsigned hh_so = WS_HH + (tile0 * 144 + 18 * xcd + 4 * sub) * 1024;
#pragma unroll
    for (int c = 0; c < 8; ++c) {
        const int tp = c >> 1, mt = c & 1;
        const f32x4 cs0 = *(LAS f32x4*)(L0 + LM + 256 + tp * 128 + g * 16), cs1 = *(LAS f32x4*)(L0 + LM + 256 + tp * 128 + 64 + g * 16);
#pragma unroll
        for (int i = 0; i < 3; ++i) { const int k = 3 * c + i; if (k < 17) LOAD_Q(0, k); else if (k > 17) LOAD_Q(1, k - 18); }
        own[mt][tp] = cat(gelu_acc(acc[mt][2 * tp], cs0, nmean[mt], c1[mt], c2[mt], ia[mt]), gelu_acc(acc[mt][2 * tp + 1], cs1, nmean[mt], c1[mt], c2[mt], ia[mt]));
        ST4(own[mt][tp], vl16, sWS, hh_so + mt * 147456, tp * 1024);
        SB();
    }
    BARV(31);
    TS(10);
    const unsigned f3_so = team * FT + F3_OFF + (xcd * 4 + w) * 128;
    const unsigned vf4 = (lane & 3) * 8;
    v2u fl;
#define P2RD(s, o) trd(((s) < 14 ? p2a + (s)*1024 : p2b + ((s)-14) * 1024) + (o))
#pragma unroll
    for (int t = 0; t < 4; ++t) { wl[t] = P2RD(t, 0); wh[t] = P2RD(t, 512); }
#pragma unroll
    for (int s = 0; s < 18; ++s) {
        if (s == 10) { WAITV(10); ST2(tagT, vone, sWS, f3_so + sub * 8, 0); }
        if (s < 12) LOAD_Q(1, s + 6);
        if (s == 14) LD2V(fl, vf4, sWS, f3_so, 0);
        const v4u wf = cat(wl[s & 3], wh[s & 3]);
        if (s == 0) { MFMA_VA0(accC[0], wf, X[0][s]); MFMA_VA0(accC[1], wf, X[1][s]); MFMA_VV0(csC, wf, ones); }
        else { MFMA_VA(accC[0], wf, X[0][s]); MFMA_VA(accC[1], wf, X[1][s]); MFMA_VV(csC, wf, ones); }
        if (s + 4 < 18) { wl[s & 3] = P2RD(s + 4, 0); wh[s & 3] = P2RD(s + 4, 512); }
        SB();
    }
    NOP8();
    asm volatile("" : "+v"(accC[0]), "+v"(accC[1]), "+v"(csC));
#pragma unroll
    for (int mt = 0; mt < 2; ++mt)
        ST2(gelu_acc(accC[mt], csC, nmean[mt], c1[mt], c2[mt], ia[mt]), vl8, sWS, WS_HH + ((tile0 + mt) * 144 + 18 * xcd + 16 + (sub >> 1)) * 1024 + (sub & 1) * 512, 0);
    TS(11);
    POLL_CHECK(2, fl, vf4, sWS, f3_so, tagT, dead);
    v4u Hf[2][12], Hc[2][2];
    v2u hc[2][2][2];
    const unsigned h_so = WS_HH + (tile0 * 144 + 18 * xcd) * 1024;
    unsigned hb[3];
#pragma unroll
    for (int i = 0; i < 3; ++i) hb[i] = h_so + ((sub + 1 + i) & 3) * 4096;
#define LOAD_H(mt, u) LD4A(Hf[mt][(u)-4], vl16, sWS, hb[((u) >> 2) - 1] + (mt)*147456, ((u)&3) * 1024)
#pragma unroll
    for (int u = 4; u < 8; ++u) { LOAD_H(0, u); LOAD_H(1, u); }
    BARL();
    TS(18);
#pragma unroll
    for (int i = 0; i < 4; ++i) glds(vwD, sW4, w4row + (17 * 32 + 8 * i) * 1152 + 256 * sub, L0 + LA + 17 * 4096 + i * 1024);
#pragma unroll
    for (int i = 0; i < 4; ++i) glds(vpD, sW4, w4row + KPOS(HEAD_PIECE(i)) * 32 * 1152 + n4c, L0 + LC + HEAD_PIECE(i) * 1024);
#pragma unroll
    for (int t = 0; t < 4; ++t) { wl[t] = trd(lap[t]); wh[t] = trd(lap[t] + 2048); }
    SB();
    const unsigned pp_so = WS_PP + (((team * 4 + sub) * 8 + 2 * w) * 8 + xcd) * 4608;
    const unsigned f4_so = team * FT + F4_OFF + (sub * 4 + w) * 256;
#define G4_MFMA(ac, mt, wf, s)                                                               \
    do {                                                                                     \
        if ((s) == 0) MFMA_VV0(ac, wf, own[mt][0]);                                          \
        else if ((s) < 4) MFMA_VV(ac, wf, own[mt][(s) < 4 ? (s) : 0]);                       \
        else if ((s) < 16) MFMA_VA(ac, wf, Hf[mt][(s) >= 4 && (s) < 16 ? (s)-4 : 0]);        \
        else MFMA_VV(ac, wf, Hc[mt][(s) >= 16 ? (s)-16 : 0]);                                \
    } while (0)
#define PART_STAGE(kk, P)                                                                    \
    do {                                                                                     \
        const int id_ = (kk) >> 1, mt_ = id_ >> 2, t_ = id_ & 3, e_ = ((kk)&1) * 2;          \
        po[(t_ & 1) * 2 + ((kk)&1)] = pk2(acc[mt_][4 * (P) + t_][e_], acc[mt_][4 * (P) + t_][e_ + 1]); \
        if (((kk)&3) == 3) ST4(po, vl16, sWS, pp_so + mt_ * 36864, (2 * (P) + (t_ >> 1)) * 1024); \
    } while (0)
    v4u po;
#pragma unroll
    for (int s = 0; s < 18; ++s) {
        if (s == 2) { WAITV(20); ST2(tagT, vone, sWS, f3_so + 32 + sub * 8, 0); }
        if (s >= 4 && s <= 10) WAITV_A2(s < 8 ? 23 : s == 8 ? 15 : s == 9 ? 14 : 11, Hf[0][s >= 4 && s <= 10 ? s - 4 : 0], Hf[1][s >= 4 && s <= 10 ? s - 4 : 0]);
        if (s == 8) LD2V(fl, vf4, sWS, f3_so, 32);
        if (s == 11) {
            POLL_CHECK(0, fl, vf4, sWS, f3_so + 32, tagT, dead);
#pragma unroll
            for (int u = 7; u < 12; ++u) asm volatile("" : "+a"(Hf[0][u]), "+a"(Hf[1][u]));
#pragma unroll
            for (int i = 0; i < 8; ++i) LD2V(hc[(i >> 1) & 1][i >> 2][i & 1], vl8, sWS, h_so + 16384 + ((i >> 1) & 1) * 147456, (i >> 2) * 1024 + (i & 1) * 512);
        }
        if (s >= 16) {
            const int u = s >= 16 ? s - 16 : 0;
            asm volatile("s_waitcnt vmcnt(%4)" : "+v"(hc[0][u][0]), "+v"(hc[0][u][1]), "+v"(hc[1][u][0]), "+v"(hc[1][u][1]) : "i"(s == 16 ? 4 : 0));
            Hc[0][u] = cat(hc[0][u][0], hc[0][u][1]); Hc[1][u] = cat(hc[1][u][0], hc[1][u][1]);
        }
#pragma unroll
        for (int t = 0; t < 4; ++t) {
            const v4u wf = cat(wl[t], wh[t]);
            G4_MFMA(acc[0][t], 0, wf, s);
            if (s < 17 && t > 0) wh[t - 1] = trd(lap[t - 1] + (s + 1) * 4096 + 2048);
            G4_MFMA(acc[1][t], 1, wf, s);
            if (s < 17) wl[t] = trd(lap[t] + (s + 1) * 4096);
            if (s < 17 && t == 3) wh[3] = trd(lap[3] + (s + 1) * 4096 + 2048);
            if (s == 17) { wl[t] = trd(lap[4 + t]); wh[t] = trd(lap[4 + t] + 2048); }
            if (s < 8 && t == 0) LOAD_H(0, s + 8);
            if (s < 8 && t == 2) LOAD_H(1, s + 8);
            SB();
        }
    }
    BARL();
    TS(12);
    glds(vpD, sW4, w4row + KPOS(14 + w) * 32 * 1152 + n4c, lw + LA);
#pragma unroll
    for (int s = 0; s < 18; ++s) {
        if (s == 10) { WAITV(0); ST2(tagT, vone, sWS, f4_so + xcd * 8, 0); }
#pragma unroll
        for (int t = 0; t < 4; ++t) {
            const v4u wf = cat(wl[t], wh[t]);
            G4_MFMA(acc[0][4 + t], 0, wf, s);
            if (s < 17 && t > 0) wh[t - 1] = trd(lap[3 + t] + (s + 1) * 4096 + 2048);
            if (s == 1 || s == 2) PART_STAGE((s - 1) * 8 + 2 * t, 0);
            G4_MFMA(acc[1][4 + t], 1, wf, s);
            if (s < 17) wl[t] = trd(lap[4 + t] + (s + 1) * 4096);
            if (s < 17 && t == 3) wh[3] = trd(lap[7] + (s + 1) * 4096 + 2048);
            if (s == 1 || s == 2) PART_STAGE((s - 1) * 8 + 2 * t + 1, 0);
            SB();
        }
    }
    BARL();
    TS(13);
    p2b = L0 + LA + vl8;
#pragma unroll
    for (int t = 0; t < 4; ++t) { wl[t] = P2RD(t, 0); wh[t] = P2RD(t, 512); }
#pragma unroll
    for (int kk = 0; kk < 16; ++kk) PART_STAGE(kk, 1);
    SB();
#pragma unroll
    for (int s = 0; s < 18; ++s) {
        const v4u wf = cat(wl[s & 3], wh[s & 3]);
        G4_MFMA(accC[0], 0, wf, s);
        G4_MFMA(accC[1], 1, wf, s);
        if (s + 4 < 18) { wl[s & 3] = P2RD(s + 4, 0); wh[s & 3] = P2RD(s + 4, 512); }
        SB();
    }
    NOP8();
    asm volatile("" : "+v"(accC[0]), "+v"(accC[1]));
#pragma unroll
    for (int mt = 0; mt < 2; ++mt) {
        v2u o; o.x = pk2(accC[mt][0], accC[mt][1]); o.y = pk2(accC[mt][2], accC[mt][3]);
        ST2(o, vl8, sWS, pp_so + mt * 36864 + 4096, 0);
    }
    TS(19);
    WAITV(2);
    ST2(tagT, vone, sWS, f4_so + 64 + xcd * 8, 0);
    WAITV(1);
    ST2(tagT, vone, sWS, f4_so + 128 + xcd * 8, 0);
    TS(14);
    {
        asm volatile("" : "+s"(w));
        const unsigned fr_so = team * FT + F4_OFF + (sub * 4 + (xcd >> 1)) * 256;
        const unsigned pr_so = WS_PP + (((team * 4 + sub) * 8 + xcd) * 8) * 4608;
        const int tileo = team * 8 + xcd;
        const unsigned vo = m * 1152 + g * 8;
        poll((lane & 7) * 8 + ((w == 0 && (lane & 8)) ? 128 : (w >> 1) * 64), sWS, fr_so, tagT, dead);
        v4u p[8], xr;
        v2u c[8], xc;
#pragma unroll
        for (int i = 0; i < 8; ++i) LD4V(p[i], vl16, sWS, pr_so + i * 4608 + w * 1024, 0);
        LD4V(xr, vl16, sWS, WS_X1F + (tileo * 18 + 4 * sub + w) * 1024, 0);
        const unsigned vc8 = w == 0 ? vl8 : OOB, vc16 = w == 0 ? vl16 : OOB, voc = w == 0 ? vo : OOB;
#pragma unroll
        for (int i = 0; i < 8; ++i) LD2V(c[i], vc8, sWS, pr_so + i * 4608 + 4096, 0);
        LD2V(xc, vc16, sWS, WS_X1F + (tileo * 18 + 16 + (sub >> 1)) * 1024 + (sub & 1) * 8, 0);
        asm volatile("s_waitcnt vmcnt(9)" : "+v"(p[0]), "+v"(p[1]), "+v"(p[2]), "+v"(p[3]), "+v"(p[4]), "+v"(p[5]), "+v"(p[6]), "+v"(p[7]), "+v"(xr));
        v4u o;
#pragma unroll
        for (int e = 0; e < 4; ++e) {
            float a0 = 0.f, a1 = 0.f;
#pragma unroll
            for (int i = 0; i < 8; ++i) { a0 += blo(p[i][e]); a1 += bhi(p[i][e]); }
            const unsigned hv = pk2(a0, a1);
            o[e] = pk2(blo(xr[e]) + blo(hv), bhi(xr[e]) + bhi(hv));
        }
        asm volatile("s_waitcnt vmcnt(0)" : "+v"(c[0]), "+v"(c[1]), "+v"(c[2]), "+v"(c[3]), "+v"(c[4]), "+v"(c[5]), "+v"(c[6]), "+v"(c[7]), "+v"(xc));
        v2u o0 = {o.x, o.y}, o1 = {o.z, o.w};
        ST2P(o0, vo, sO, tileo * 16 * 1152 + (128 * sub + 32 * w) * 2, 0);
        ST2P(o1, vo, sO, tileo * 16 * 1152 + (128 * sub + 32 * w) * 2, 32);
        v2u oc;
#pragma unroll
        for (int e = 0; e < 2; ++e) {
            float a0 = 0.f, a1 = 0.f;
#pragma unroll
            for (int i = 0; i < 8; ++i) { a0 += blo(c[i][e]); a1 += bhi(c[i][e]); }
            const unsigned hv = pk2(a0, a1);
            oc[e] = pk2(blo(xc[e]) + blo(hv), bhi(xc[e]) + bhi(hv));
        }
        ST2P(oc, voc, sO, tileo * 16 * 1152 + (512 + 16 * sub) * 2, 0);
        if (xcd == 0 && sub == 0 && w == 0) ST2(tagT, vone, sWS, team * FT, 0);
    }
    while (__builtin_amdgcn_s_memrealtime() - tpad < 6000ull) __builtin_amdgcn_s_sleep(1);
    asm volatile(".Lmx_end:");
}

extern "C" void kernel_launch(void* const* d_in, const int* in_sizes, int n_in, void* d_out, int out_size, void* d_ws, size_t ws_size,
                              hipStream_t stream) {
    mixer_kernel<<<256, 256, 0, stream>>>((const u16*)d_in[0], (const u16*)d_in[1], (const u16*)d_in[2], (const u16*)d_in[3],
                                          (const u16*)d_in[4], (u16*)d_out, (unsigned char*)d_ws);
}
```

```cpp
#include <hip/hip_runtime.h>
#include <stdint.h>

typedef unsigned short u16;
typedef unsigned v4u __attribute__((ext_vector_type(4)));
typedef unsigned v2u __attribute__((ext_vector_type(2)));
typedef short s16x4 __attribute__((ext_vector_type(4)));
typedef float f32x4 __attribute__((ext_vector_type(4)));
typedef float f32x2 __attribute__((ext_vector_type(2)));
typedef __bf16 bf16x2 __attribute__((ext_vector_type(2)));
#define LAS __attribute__((address_space(3)))
#define DI __device__ __forceinline__

#define WS_HF 0x180000
#define WS_X1F (WS_HF + 1179648)
#define WS_ST (WS_X1F + 1179648)
#define WS_CS (WS_ST + 49152)
#define WS_HH (WS_CS + 24576)
#define WS_PP (WS_HH + 9437184)
#define WS_TOTAL (WS_PP + 10485760)
#define WS_OFFSET(sz) (((sz)-WS_TOTAL) & ~(size_t)0x1fffff)
constexpr int FT = 0x30000, F1_OFF = 0x1000, F2_OFF = 0x4000, F3_OFF = 0x10000, F4_OFF = 0x1c000, F5_OFF = 0x28000;
constexpr int LA = 0, LB = 73728, LC = 147456, LM = 161792, LS = LM + 768;
constexpr unsigned SPIN_CAP = 100000;

DI v4u mk_srd(const void* p, unsigned bytes) {
    unsigned long long a = (unsigned long long)p;
    v4u s; s.x = (unsigned)a; s.y = (unsigned)(a >> 32) & 0xffffu; s.z = bytes; s.w = 0x20000u; return s;
}
DI void glds(unsigned voff, v4u srd, unsigned soff, unsigned lds) {
    asm volatile("s_mov_b32 m0, %3\n\ts_nop 0\n\tbuffer_load_dwordx4 %0, %1, %2 offen lds" ::"v"(voff), "s"(srd), "s"(soff), "s"(lds));
}
#define LD4V(d, voff, srd, soff, imm) asm volatile("buffer_load_dwordx4 %0, %1, %2, %3 offen offset:%4 sc1" : "=v"(d) : "v"(voff), "s"(srd), "s"(soff), "i"(imm))
#define LD2V(d, voff, srd, soff, imm) asm volatile("buffer_load_dwordx2 %0, %1, %2, %3 offen offset:%4 sc1" : "=v"(d) : "v"(voff), "s"(srd), "s"(soff), "i"(imm))
#define LD2P(d, voff, srd, soff, imm) asm volatile("buffer_load_dwordx2 %0, %1, %2, %3 offen offset:%4" : "=v"(d) : "v"(voff), "s"(srd), "s"(soff), "i"(imm))
#define TOUCH(d, voff, srd, soff) asm volatile("buffer_load_dword %0, %1, %2, %3 offen sc1" : "+v"(d) : "v"(voff), "s"(srd), "s"(soff))
#define WARM(voff, srd, soff) asm volatile("buffer_load_dwordx4 %0, %1, %2 offen lds" ::"v"(voff), "s"(srd), "s"(soff))
#define ST4(d, voff, srd, soff, imm) asm volatile("buffer_store_dwordx4 %0, %1, %2, %3 offen offset:%4 sc1\n\ts_nop 1" ::"v"(d), "v"(voff), "s"(srd), "s"(soff), "i"(imm))
#define ST2(d, voff, srd, soff, imm) asm volatile("buffer_store_dwordx2 %0, %1, %2, %3 offen offset:%4 sc1" ::"v"(d), "v"(voff), "s"(srd), "s"(soff), "i"(imm))
#define ST4X(d, voff, srd, soff, imm) asm volatile("buffer_store_dwordx4 %0, %1, %2, %3 offen offset:%4 sc1 ; POLL\n\ts_nop 1" ::"v"(d), "v"(voff), "s"(srd), "s"(soff), "i"(imm))
#define ST2X(d, voff, srd, soff, imm) asm volatile("buffer_store_dwordx2 %0, %1, %2, %3 offen offset:%4 sc1 ; POLL" ::"v"(d), "v"(voff), "s"(srd), "s"(soff), "i"(imm))
#define ST2P(d, voff, srd, soff, imm) asm volatile("buffer_store_dwordx2 %0, %1, %2, %3 offen offset:%4" ::"v"(d), "v"(voff), "s"(srd), "s"(soff), "i"(imm))
#define WAITV(n) asm volatile("s_waitcnt vmcnt(%0)" ::"i"(n) : "memory")
#define WAITV_V2(n, a, b) asm volatile("s_waitcnt vmcnt(%2)" : "+v"(a), "+v"(b) : "i"(n))
#define WAITV_V1(n, a) asm volatile("s_waitcnt vmcnt(%1)" : "+v"(a) : "i"(n))
#define BARV(n) asm volatile("s_waitcnt vmcnt(%0) lgkmcnt(0)\n\ts_barrier" ::"i"(n) : "memory")
#define BARW(n) asm volatile("s_waitcnt vmcnt(%0)\n\ts_barrier" ::"i"(n) : "memory")
#define BARE() asm volatile("s_barrier" ::: "memory")
#define MARK(name, i) asm volatile("; MARK " #name " %0" ::"i"(i))
#define NEED(name, i) asm volatile("; NEED " #name " %0" ::"i"(i))
#define VM(name, i) (vm_##name[i])
constexpr int vm_g1[5] = {11, 4, 4, 4, 0};
constexpr int vm_ep[1] = {20};
constexpr int vm_xr[1] = {0};
constexpr int vm_h[18] = {10, 10, 10, 10, 10, 10, 10, 10, 10, 12, 14, 16, 17, 17, 18, 19, 20, 19};
constexpr int vm_x[18] = {8, 8, 8, 8, 8, 8, 8, 8, 8, 8, 8, 8, 8, 8, 6, 4, 2, 0};
constexpr int vm_sa[1] = {1};
constexpr int vm_sb[1] = {6};
constexpr int vm_fl[2] = {2, 0};
constexpr int vm_hh[18] = {0, 0, 0, 0, 12, 10, 8, 9, 11, 13, 18, 16, 14, 12, 10, 8, 4, 0};
constexpr int vm_p0[1] = {0};
constexpr int vm_p1[1] = {5};
constexpr int vm_fr[1] = {3};
constexpr unsigned CODE_BYTES = 53504;
#define SB() __builtin_amdgcn_sched_barrier(0)
#define MFMA_VV(c, a, b) asm volatile("v_mfma_f32_16x16x32_bf16 %0, %1, %2, %0" : "+v"(c) : "v"(a), "v"(b))
#define MFMA_VV0(c, a, b) asm volatile("v_mfma_f32_16x16x32_bf16 %0, %1, %2, 0" : "=&v"(c) : "v"(a), "v"(b))
#define GL_INC(voff, srd, so, lw, limm, sinc) asm volatile("s_add_u32 m0, %3, %4\n\ts_add_u32 %0, %0, %5\n\tbuffer_load_dwordx4 %1, %2, %0 offen lds" : "+s"(so) : "v"(voff), "s"(srd), "s"(lw), "i"(limm), "i"(sinc) : "scc")
#define GL_Q(voff, srd, so, lw, limm, off) asm volatile("s_add_u32 m0, %3, %4\n\ts_nop 0\n\tbuffer_load_dwordx4 %0, %1, %2 offen offset:%5 lds" ::"v"(voff), "s"(srd), "s"(so), "s"(lw), "i"((limm) - (off)), "i"(off) : "scc")
#define OPQ(x) asm volatile("" : "+v"(x))
#define NOP8() asm volatile("s_nop 7\n\ts_nop 1" ::: "memory")

DI v2u trd(unsigned addr) { return __builtin_bit_cast(v2u, __builtin_amdgcn_ds_read_tr16_b64_v4i16((LAS s16x4*)addr)); }
DI v4u cat(v2u a, v2u b) { v4u r; r.x = a.x; r.y = a.y; r.z = b.x; r.w = b.y; return r; }
DI unsigned pk2(float a, float b) { bf16x2 p; p.x = (__bf16)a; p.y = (__bf16)b; return __builtin_bit_cast(unsigned, p); }
DI float blo(unsigned u) { return __uint_as_float(u << 16); }
DI float bhi(unsigned u) { return __uint_as_float(u & 0xffff0000u); }
DI float xsum4(float x) {
    auto s = __builtin_amdgcn_permlane16_swap(__float_as_uint(x), __float_as_uint(x), false, false);
    x = __uint_as_float(s[0]) + __uint_as_float(s[1]);
    auto t = __builtin_amdgcn_permlane32_swap(__float_as_uint(x), __float_as_uint(x), false, false);
    return __uint_as_float(t[0]) + __uint_as_float(t[1]);
}
constexpr float GK1 = -2.3022081986f, GK2 = -0.1029432397f;
#define POLL_CHECK(n, f, voff, srd, soff, tag, budget)                                                                  \
    asm volatile("s_waitcnt vmcnt(%6)\n"                                                                                \
                 ".Lpoll%=:\n\tv_cmp_ne_u64 vcc, %0, %5\n\ts_cbranch_vccz .Ldone%=\n\ts_cmp_eq_u32 %1, 0\n\ts_cbranch_scc1 .Ldone%=\n\t" \
                 "s_sub_u32 %1, %1, 1\n\ts_sleep 1\n\tbuffer_load_dwordx2 %0, %2, %3, %4 offen sc1 ; POLL\n\ts_waitcnt vmcnt(0)\n\ts_branch .Lpoll%=\n" \
                 ".Ldone%=:"                                                                                            \
                 : "+v"(f), "+s"(budget) : "v"(voff), "s"(srd), "s"(soff), "v"(tag), "i"(n) : "vcc", "scc", "memory")
#define POLL(voff, srd, soff, tag, budget) do { v2u f_; LD2V(f_, voff, srd, soff, 0); POLL_CHECK(0, f_, voff, srd, soff, tag, budget); } while (0)

#define LDS_WAIT(r, addr, k, budget)                                                                                    \
    asm volatile(".Lpoll%=:\n\tv_cmp_gt_u32 vcc, %3, %0\n\ts_cbranch_vccz .Ldone%=\n\ts_cmp_eq_u32 %1, 0\n\ts_cbranch_scc1 .Ldone%=\n\t" \
                 "s_sub_u32 %1, %1, 1\n\ts_sleep 1\n\tds_read_b32 %0, %2\n\ts_waitcnt lgkmcnt(0)\n\ts_branch .Lpoll%=\n.Ldone%=:"        \
                 : "+v"(r), "+s"(budget) : "v"(addr), "s"((unsigned)(k)) : "vcc", "scc", "memory")
#define LDS_RD(addr) (*(volatile LAS unsigned*)(addr))
#define LDS_SET(addr, k) (*(volatile LAS unsigned*)(addr) = (k))
#define AWAIT(addr, k) do { unsigned r_ = LDS_RD(addr); LDS_WAIT(r_, addr, k, dead); } while (0)

DI v4u gelu8(f32x4 a0, f32x4 a1, f32x4 cs0, f32x4 cs1, float nm, float c1, float c2, float ia) {
    f32x2 v[4] = {{a0[0], a0[1]}, {a0[2], a0[3]}, {a1[0], a1[1]}, {a1[2], a1[3]}}, u[4];
    const f32x2 cs[4] = {{cs0[0], cs0[1]}, {cs0[2], cs0[3]}, {cs1[0], cs1[1]}, {cs1[2], cs1[3]}};
#define G8(stmt) _Pragma("unroll") for (int i = 0; i < 4; ++i) { stmt; } SB()
    G8(v[i] = __builtin_elementwise_fma(cs[i], f32x2{nm, nm}, v[i]));
    G8(u[i] = v[i] * v[i]);
    G8(u[i] = __builtin_elementwise_fma(u[i], f32x2{c2, c2}, f32x2{c1, c1}));
    G8(u[i] = v[i] * u[i]);
    G8(u[i].x = __builtin_amdgcn_exp2f(u[i].x); u[i].y = __builtin_amdgcn_exp2f(u[i].y));
    G8(u[i] = __builtin_elementwise_fma(u[i], f32x2{ia, ia}, f32x2{ia, ia}));
    G8(u[i].x = __builtin_amdgcn_rcpf(u[i].x); u[i].y = __builtin_amdgcn_rcpf(u[i].y));
    G8(u[i] = v[i] * u[i]);
    v4u o;
    G8(o[i] = pk2(u[i].x, u[i].y));
    return o;
}

__global__ __launch_bounds__(512, 2) void standalone__mixer_s_mlp__sampling_kernel(const u16* __restrict__ x, const u16* __restrict__ w1, const u16* __restrict__ w2,
                                                       const u16* __restrict__ w3, const u16* __restrict__ w4, u16* __restrict__ out,
                                                       unsigned char* __restrict__ ws) {
    __shared__ __attribute__((aligned(1024))) unsigned char smem[163840];
    asm volatile("" ::"s"(x), "s"(w1), "s"(w2), "s"(w3), "s"(w4), "s"(out), "s"(ws));
    const int b = blockIdx.x, xcd = b & 7, j = b >> 3;
    const int lane = threadIdx.x & 63;
    const int w8 = __builtin_amdgcn_readfirstlane(threadIdx.x >> 6);
    int w = w8 & 3;
    const unsigned vl16 = lane * 16;
    const unsigned L0 = (unsigned)(size_t)(LAS unsigned char*)smem, lw = L0 + w * 1024;
    const bool act = j < 18;
    constexpr unsigned OOB = 0x80000000u;
    const int r8 = lane >> 3, pos = lane & 7;
    const int rh = (j * 29) >> 8, c9 = j - 9 * rh;
    const bool late = j >= 18 && j < 30, publisher = (0xc003300cu >> j) & 1;
    {
        unsigned nb = w8 >= 4 ? (j < 18 ? 8 : late ? 3 : 1) + publisher : 0;
        asm volatile(".Lpoll%=:\n\ts_cmp_eq_u32 %0, 0\n\ts_cbranch_scc1 .Ldone%=\n\ts_barrier\n\ts_sub_u32 %0, %0, 1\n\ts_branch .Lpoll%=\n.Ldone%=:" : "+s"(nb)::"scc", "memory");
    }
    if (__builtin_expect(w8 >= 4, 0)) {
        const int team = j & 7, sub = j >> 3, m = lane & 15, g = lane >> 4;
        const v4u sW3 = mk_srd(w3, 5308416), sW4 = mk_srd(w4, 5308416), sWS = mk_srd(ws, WS_TOTAL);
        const unsigned swzc = (((pos >> 1) ^ ((r8 >> 1) & 3)) << 5) + (pos & 1) * 16;
        const unsigned vwD = r8 * 1152 + swzc, vwF = r8 * 9216 + swzc;
        const unsigned vpD = (lane >> 1) * 1152 + (lane & 1) * 16, vpF = (lane >> 1) * 9216 + (lane & 1) * 16;
        const unsigned n3 = (576 * xcd + 128 * sub) * 2, n3c = (576 * xcd + 512 + 16 * sub) * 2;
        const unsigned aC = L0 + LS + (lane & 3) * 4, oL = L0 + LS + 16 + w * 4, oS = oL + 16;
        unsigned dead = SPIN_CAP;
#define LANDED(n, k) do { WAITV(n); LDS_SET(oL, k); } while (0)
#define L3_C(p, slot) glds(vpF, sW3, (p)*32 * 9216 + n3c, L0 + LC + (slot)*1024)
#define L3_P(P, i) glds(vwF, sW3, (8 * w + 32 * (i)) * 9216 + n3 + (P)*128, lw + ((P) ? LB : LA) + (i)*4096)
        __builtin_amdgcn_s_sleep(4);
        if (late) {
#pragma nounroll
            for (int i = 0; i < 4; ++i) { const int p = w + 4 * i < 14 ? w + 4 * i : w + 8; L3_C(p, p); }
            L3_P(1, 9); L3_P(0, 10); L3_P(1, 10); L3_P(0, 11); L3_P(1, 11);
            LANDED(5, 7);
#pragma nounroll
            for (int i = 12; i < 18; ++i) { L3_P(0, i); L3_P(1, i); }
        }
        LANDED(0, 8);
        const unsigned w4row = 576 * 1152 * xcd;
#define KPOS_RT(u) ((u) < 16 ? 4 * ((sub + ((0xfa5fa500u >> (2 * ((u)&15))) & 3)) & 3) + ((0xeee444e4u >> (2 * ((u)&15))) & 3) : (u))
#define W4_AB(u0, u1, pace)                                                                                                \
    _Pragma("nounroll") for (int u_ = (u0); u_ < (u1); ++u_) {                                                             \
        const unsigned so_ = w4row + (KPOS_RT(u_) * 32 + 8 * w) * 1152 + 256 * sub;                                        \
        glds(vwD, sW4, so_, lw + LA + u_ * 4096); __builtin_amdgcn_s_sleep(pace);                                          \
        glds(vwD, sW4, so_ + 128, lw + LB + u_ * 4096); __builtin_amdgcn_s_sleep(pace);                                    \
    }
#define W4_C(u) glds(vpD, sW4, w4row + KPOS_RT(u) * 32 * 1152 + (512 + 16 * sub) * 2, L0 + LC + ((u) < 10 ? (u) + 4 : (u)-10) * 1024)
        AWAIT(aC, 11);
        L3_C(14 + w, w);
        v2u tg;
        {
            v2u st[4];
#pragma unroll
            for (int i = 0; i < 4; ++i)
                LD2V(st[i], ((i & 1) == 0 ? g * 128 + m * 8 : g < 2 ? 512 + g * 128 + m * 8 : OOB), sWS, WS_ST + (team * 8 + 2 * w + (i >> 1)) * 768, 0);
            LD2V(tg, 0u, sWS, team * FT, 0);
            asm volatile("s_waitcnt vmcnt(0)" : "+v"(st[0]), "+v"(st[1]), "+v"(st[2]), "+v"(st[3]), "+v"(tg));
            tg.x += 1;
#pragma unroll
            for (int mt = 0; mt < 2; ++mt) {
                const float s1 = xsum4(__uint_as_float(st[2 * mt].x) + __uint_as_float(st[2 * mt + 1].x));
                const float s2 = xsum4(__uint_as_float(st[2 * mt].y) + __uint_as_float(st[2 * mt + 1].y));
                const float mean = s1 * (1.0f / 576.0f);
                const float var = __builtin_fmaxf(__builtin_fmaf(-mean, mean, s2 * (1.0f / 576.0f)), 0.f) + 1e-5f;
                if (g == 0) *(LAS f32x2*)(L0 + LM + 1024 + (w * 32 + mt * 16 + m) * 8) = f32x2{-mean, __builtin_amdgcn_rsqf(var)};
            }
        }
        LDS_SET(oL, 9);
        W4_AB(0, 5, 1);
        AWAIT(aC, 12);
        W4_C(w);
        W4_AB(5, 10, 1);
        if (w == 0) {
            POLL((lane < 8 ? lane : 0) * 128, sWS, team * FT + F5_OFF + (xcd * 4 + sub) * 1024, tg, dead);
            v4u cs;
            asm volatile("buffer_load_dwordx4 %0, %1, %2, %3 offen sc1\n\ts_waitcnt vmcnt(0)" : "=&v"(cs) : "v"(vl16), "s"(sWS), "s"(WS_CS + (xcd * 4 + sub) * 768));
            if (lane < 32) *(LAS v4u*)(L0 + LM + lane * 16) = cs;
        }
        LDS_SET(oS, 1);
        AWAIT(aC, 13);
        W4_C(4 + w);
        W4_C(8 + (w & 1));
        W4_AB(10, 14, 3);
        AWAIT(aC, 14);
        LANDED(0, 10);
        W4_C(10 + w);
        W4_AB(14, 18, 3);
        LANDED(0, 11);
        AWAIT(aC, 15);
        W4_C(14 + w);
        LANDED(0, 12);
        return;
    }
    const v4u sX = mk_srd(x, 1179648), sW1 = mk_srd(w1, 663552);
    const unsigned vx0 = r8 * 1152 + ((pos ^ (r8 >> 1)) << 4), vx1 = 8 * 1152 + r8 * 1152 + ((pos ^ ((r8 >> 1) | 4)) << 4);
    const unsigned vw1 = r8 * 1152 + (((pos >> 1) ^ (((r8 >> 1) & 1) | ((w & 1) << 1))) << 5) + (pos & 1) * 16, vw1b = vw1 + 32 * 1152;
    const unsigned xs0 = (xcd * 128 + rh * 64 + 16 * w) * 1152, ws0 = 8 * w * 1152 + c9 * 128, lx = L0 + LB + 2 * w * 9216;
    unsigned ln = (c9 + xcd >= 9 ? c9 + xcd - 9 : c9 + xcd) * 128;
#define G1_X(u) do { const unsigned so_ = xs0 + ln; GL_Q(vx0, sX, so_, lx, (u)*1024, 0); GL_Q(vx1, sX, so_, lx, 9216 + (u)*1024, 0); } while (0)
#define G1_W(u) do { const unsigned so_ = ws0 + ln * 576; GL_Q(vw1, sW1, so_, lw, LA + (u)*8192, 0); GL_Q(vw1b, sW1, so_, lw, LA + (u)*8192 + 4096, 0); \
                     ln = ln == 1024 ? 0 : ln + 128; } while (0)
    const int wv = (j - 18) * 4 + w;
    if (__builtin_expect(act, 1)) {
#pragma unroll
        for (int u = 0; u < 2; ++u) { G1_X(u); G1_W(u); }
        MARK(g1, 0);
    } else if (j < 30) {
        asm volatile("s_mov_b32 m0, %0\n\ts_nop 0" ::"s"(lw + LA));
#pragma unroll
        for (int i = 0; i < 3; ++i) WARM(vl16, sX, xcd * 147456 + (wv + 48 * i) * 1024);
        unsigned pr = (wv + 81 * xcd) * 1024;
#pragma unroll
        for (int i = 0; i < 14; ++i) { WARM(vl16, sW1, pr); pr += 48 * 1024; pr = pr < 663552 ? pr : pr - 663552; }
    }
    SB();
    {
        unsigned long long pc;
        const unsigned len = CODE_BYTES;
        asm volatile("s_getpc_b64 %0" : "=s"(pc));
        v4u sC; sC.x = (unsigned)pc; sC.y = (unsigned)(pc >> 32) & 0xffffu; sC.z = len; sC.w = 0x20000u;
        glds(vl16, sC, (j * 4 + w) * 1024, lw + LC);
    }
    const int team = j & 7, sub = j >> 3;
    const v4u sW2 = mk_srd(w2, 663552), sW3 = mk_srd(w3, 5308416), sW4 = mk_srd(w4, 5308416), sO = mk_srd(out, 1179648), sWS = mk_srd(ws, WS_TOTAL);
    v2u tagR, tagT;
    if (threadIdx.x < 16) LDS_SET(L0 + LS + threadIdx.x * 4, 0);
    SB();
#define TAKE_EPOCHS(n) do { WAITV_V2(n, tagR, tagT); tagR.x += 1; tagT.x += 1; } while (0)
    const unsigned vl8 = lane * 8;
    const int m = lane & 15, g = lane >> 4, q = m >> 2;
    const unsigned vone = lane == 0 ? 0u : OOB, vf18 = (lane < 18 ? lane : 0) * 128, vf12 = (lane < 12 ? lane : 0) * 128, vf4 = (lane < 4 ? lane : 0) * 128;
    const unsigned swzc = (((pos >> 1) ^ ((r8 >> 1) & 3)) << 5) + (pos & 1) * 16;
    const unsigned vwD = r8 * 1152 + swzc, vwF = r8 * 9216 + swzc;
    const unsigned vpF = (lane >> 1) * 9216 + (lane & 1) * 16;
    const unsigned n3 = (576 * xcd + 128 * sub) * 2, n3c = (576 * xcd + 512 + 16 * sub) * 2;
    unsigned dead = SPIN_CAP, chk;
    const unsigned aL = L0 + LS + 16 + (lane & 3) * 4, aS = aL + 16, oC = L0 + LS + w * 4;
    unsigned lac = L0 + LC + vl8;
    OPQ(lac);
    unsigned lap[8];
    {
        const int r4 = 4 * (g & 1) + q, xp = (r4 >> 1) & 3;
#pragma unroll
        for (int t = 0; t < 4; ++t) {
            lap[t] = L0 + LA + (g >> 1) * 1024 + r4 * 128 + ((t ^ xp) << 5) + (lane & 3) * 8;
            lap[4 + t] = lap[t] + (LB - LA);
            OPQ(lap[4 + t]);
        }
    }
    v4u ones = {0x3f803f80u, 0x3f803f80u, 0x3f803f80u, 0x3f803f80u};
    OPQ(ones);
#define HEAD_PIECE(i) ((w + 4 * (i) < 14) ? w + 4 * (i) : w + 8)
    unsigned so30 = (8 * w - 32) * 9216 + n3, so31 = so30 + 128;
#define W3_P0(i) GL_INC(vwF, sW3, so30, lw, LA + (i)*4096, 32 * 9216)
#define W3_P1(i) GL_INC(vwF, sW3, so31, lw, LB + (i)*4096, 32 * 9216)
#define WARM_W3(c) WARM(((c) < 576 ? vl16 : r8 * 9216 + pos * 16), sW3, ((c) < 576 ? (c)*9216 : ((c)-576) * 8 * 9216 + 1024) + 576 * 2 * xcd)
#define W3_P2(i) glds(vpF, sW3, HEAD_PIECE(i) * 32 * 9216 + n3c, L0 + LC + HEAD_PIECE(i) * 1024)

    if (__builtin_expect(act, 1)) {
        LD2V(tagR, 0u, sWS, xcd * FT, 0);
        LD2V(tagT, 0u, sWS, team * FT, 0);
        MARK(ep, 0);
        const int swz = (m >> 1) & 7;
        const unsigned xrow = L0 + LB + (2 * w + (m >> 3)) * 9216 + (m & 7) * 128;
        unsigned xa0 = xrow + ((g ^ swz) << 4), xa1 = xrow + ((g ^ swz ^ 4) << 4);
        OPQ(xa0); OPQ(xa1);
        G1_X(2); G1_W(2);
        SB();
        unsigned la1[4];
        {
            const int x1 = (q >> 1) | ((g & 1) << 1);
#pragma unroll
            for (int t = 0; t < 4; ++t) la1[t] = L0 + LA + g * 1024 + q * 128 + ((t ^ w ^ x1) << 5) + (lane & 3) * 8;
        }
        unsigned hvo[2][2], hso[2];
        bool hin[2][2], hsplit[2];
        {
            const int tok = 16 * (w & 1) + m, batch = xcd * 4 + rh * 2 + (w >> 1);
#pragma unroll
            for (int pp = 0; pp < 2; ++pp) {
                const int Te = 4 * c9 + 2 * ((w >> 1) ^ pp);
                const int sh = Te < 9 ? 1 : Te < 18 ? -1 : Te < 27 ? 2 : -2;
                hsplit[pp] = Te == 8 || Te == 26;
                const int d0 = tok + sh, d1 = hsplit[pp] ? tok - sh : d0, sc = hsplit[pp] ? 8 : 16;
                hso[pp] = WS_HF + (batch * 36 + (Te >> 1)) * 1024;
                hin[pp][0] = d0 >= 0 && d0 < 32; hin[pp][1] = d1 >= 0 && d1 < 32;
                hvo[pp][0] = ((d0 >> 4) & 1) * 18432 + (g * 16 + (d0 & 15)) * sc;
                hvo[pp][1] = ((d1 >> 4) & 1) * 18432 + (g * 16 + (d1 & 15)) * sc;
                OPQ(hvo[pp][0]); OPQ(hvo[pp][1]);
            }
        }
        G1_X(3);
        SB();
        unsigned csa[4];
#pragma unroll
        for (int t = 0; t < 4; ++t) { csa[t] = L0 + LM + (t ^ w) * 64 + g * 16; OPQ(csa[t]); OPQ(la1[t]); }
        G1_W(3);
        MARK(g1, 1);
        f32x4 acc[4], cs[4], acs, asum, agr;
        v2u wl[3][4], wh[3][4];
        v4u xf[3];
#define G1_RDX(s) xf[(s) % 3] = *(LAS v4u*)(((s)&1 ? xa1 : xa0) + ((s) >> 1) * 1024)
#define G1_RDL(s, t) wl[(s) % 3][t] = trd(la1[t] + (s)*4096)
#define G1_RDH(s, t) wh[(s) % 3][t] = trd(la1[t] + (s)*4096 + 512)
        NEED(g1, 0); BARV(VM(g1, 0));
#pragma unroll
        for (int s = 0; s < 2; ++s) {
            G1_RDX(s);
#pragma unroll
            for (int t = 0; t < 4; ++t) { G1_RDL(s, t); G1_RDH(s, t); }
        }
#pragma unroll
        for (int s = 0; s < 18; ++s) {
            if ((s & 3) == 2) {
                NEED(g1, (s + 2) >> 2); BARW(VM(g1, (s + 2) >> 2));
                if (s == 6) { NEED(ep, 0); TAKE_EPOCHS(VM(ep, 0)); }
            }
            if (s == 16) {
                asm volatile("s_waitcnt lgkmcnt(2)\n\ts_barrier" ::: "memory");
#pragma unroll
                for (int t = 0; t < 4; ++t) cs[t] = *(LAS f32x4*)csa[t];
            }
            if (s < 10) { if (s & 1) G1_W(4 + (s >> 1)); else G1_X(4 + (s >> 1)); if ((s & 3) == 3 || s == 9) MARK(g1, 2 + (s >> 2)); }
            const v4u xc = xf[s % 3];
            if (s < 16) G1_RDX(s + 2);
            if (s == 17) { MFMA_VV(asum, ones, xc); MFMA_VV(agr, xc, xc); }
#pragma unroll
            for (int t = 0; t < 4; ++t) {
                const v4u wf = cat(wl[s % 3][t], wh[s % 3][t]);
                if (s == 0) MFMA_VV0(acc[t], wf, xc); else MFMA_VV(acc[t], wf, xc);
                if (s == 15 && t == 3 && m == 0) *(LAS f32x4*)(L0 + LM + w * 64 + g * 16) = acs;
                if (s < 16) G1_RDL(s + 2, t);
                if (t == 0 && s < 16) { if (s == 0) MFMA_VV0(acs, wf, ones); else MFMA_VV(acs, wf, ones); }
                if (t == 1 && s < 17) { if (s == 0) MFMA_VV0(asum, ones, xc); else MFMA_VV(asum, ones, xc); }
                if (t == 2 && s < 17) { if (s == 0) MFMA_VV0(agr, xc, xc); else MFMA_VV(agr, xc, xc); }
                if (s < 16) G1_RDH(s + 2, t);
                if (s == 15 && t == 2) OPQ(acs);
                SB();
            }
            if (s >= 16) {
#pragma unroll
                for (int t = 0; t < 4; ++t) MFMA_VV(cs[t], cat(wl[s % 3][t], wh[s % 3][t]), ones);
                SB();
            }
        }
        asm volatile("" : "+v"(asum), "+v"(agr));
        const float dg = (m & 2) ? ((m & 1) ? agr[3] : agr[2]) : ((m & 1) ? agr[1] : agr[0]);
        const float ssq = xsum4(g == q ? dg : 0.f);
        const float mean = asum[0] * (1.0f / 576.0f);
        const float var = __builtin_fmaxf(__builtin_fmaf(-mean, mean, ssq * (1.0f / 576.0f)), 0.f) + 1e-5f;
        const float rs = __builtin_amdgcn_rsqf(var);
        float k1 = rs * GK1, k2 = rs * rs * rs * GK2, k3 = var * rs, k0 = -mean;
        asm volatile("" : "+v"(k0), "+v"(k1), "+v"(k2), "+v"(k3), "+v"(acc[0]), "+v"(acc[1]), "+v"(acc[2]), "+v"(acc[3]), "+v"(cs[0]), "+v"(cs[1]), "+v"(cs[2]), "+v"(cs[3]));
        SB();
        const unsigned fb = xcd * FT + (rh * 2 + (w >> 1)) * 2304, fi = (c9 * 2 + (w & 1)) * 128;
        {
#pragma unroll
            for (int pp = 0; pp < 2; ++pp) {
                const v4u o = gelu8(acc[2 * pp], acc[2 * pp + 1], cs[2 * pp], cs[2 * pp + 1], k0, k1, k2, k3);
                const v2u oa = {o.x, o.y}, ob = {o.z, o.w};
                v2u lo = (w & 1) ? ob : oa, hi = (w & 1) ? oa : ob;
                lo = hin[pp][0] ? lo : v2u{0u, 0u};
                hi = hin[pp][1] ? hi : v2u{0u, 0u};
                if (hsplit[pp]) { ST2X(lo, hvo[pp][0], sWS, hso[pp], 0); ST2X(hi, hvo[pp][1], sWS, hso[pp], 512); }
                else ST4X(cat(lo, hi), hvo[pp][0], sWS, hso[pp], 0);
            }
            WAITV(0);
            ST2(tagR, vone, sWS, fb + F1_OFF + fi, 0);
        }
        BARE();
    } else if (j < 30) {
        const int gi = j - 18, rh2 = (gi * 43) >> 8, c6 = gi - 6 * rh2, odd = c6 & 1;
        const unsigned ca = (96 * c6 + 32 * odd) * 2, cb = (96 * c6 + 64 - 64 * odd) * 2;
        const int tm2 = (xcd + 1) & 7;
        const int tile = tm2 * 8 + rh2 * 4 + w;
        v2u xres[6];
        {
            unsigned so;
            {
                unsigned pi = 2 * xcd + rh2;
#pragma unroll
                for (int i = 0; i < 18; ++i) { glds(vwD, sW2, (8 * w + 32 * pi) * 1152 + ca, lw + LA + pi * 4096); pi = pi == 17 ? 0 : pi + 1; }
            }
            const unsigned vb = (lane >> 2) * 1152 + ((((lane >> 1) ^ (lane >> 4)) & 1) << 5) + (lane & 1) * 16;
            so = (16 * w - 64) * 1152 + cb;
#pragma unroll
            for (int i = 0; i < 9; ++i) GL_INC(vb, sW2, so, lw, LB + 36864 + i * 4096, 64 * 1152);
            WAITV(24);
#pragma unroll
            for (int t = 0; t < 6; ++t) LD2P(xres[t], m * 1152 + g * 8, sX, tile * 16 * 1152 + (t < 4 ? ca : cb), (t & 3) * 32);
            LD2V(tagR, 0u, sWS, tm2 * FT, 0);
            LD2V(tagT, 0u, sWS, team * FT, 0);
            MARK(xr, 0);
            NEED(xr, 0);
            asm volatile("s_waitcnt vmcnt(%8)" : "+v"(xres[0]), "+v"(xres[1]), "+v"(xres[2]), "+v"(xres[3]), "+v"(xres[4]), "+v"(xres[5]), "+v"(tagR), "+v"(tagT) : "i"(VM(xr, 0)));
            tagR.x += 1; tagT.x += 1;
        }
        unsigned lb2[2];
#pragma unroll
        for (int t = 0; t < 2; ++t) { lb2[t] = L0 + LB + 36864 + (4 * g + q) * 64 + ((t ^ (g & 1)) << 5) + (lane & 3) * 8; OPQ(lb2[t]); }
#define G2_RD(s, t) do { if ((t) < 4) { wl[(s)&1][t] = trd(lap[(t)&3] + (s)*4096); wh[(s)&1][t] = trd(lap[(t)&3] + (s)*4096 + 2048); } \
                         else { wl[(s)&1][t] = trd(lb2[(t)&1] + (s)*2048); wh[(s)&1][t] = trd(lb2[(t)&1] + (s)*2048 + 1024); } } while (0)
        v2u wl[2][6], wh[2][6];
        BARV(0);
#pragma unroll
        for (int t = 0; t < 6; ++t) G2_RD(0, t);
        const unsigned fb = tm2 * FT, bt2 = rh2 * 2 + (w >> 1);
        POLL(vf18, sWS, fb + F1_OFF + bt2 * 2304, tagR, dead);
        v4u hf[18];
        v2u hs[4];
        f32x4 acc[6];
        const unsigned hso0 = WS_HF + tile * 18432;
#define G2_LD(s)                                                                                                   \
    do {                                                                                                           \
        if ((s) == 4 || (s) == 13) { LD2V(hs[(s) == 4 ? 0 : 2], vl8, sWS, hso0 + ((s) & ~3) * 1024, ((s)&3) * 1024); LD2V(hs[(s) == 4 ? 1 : 3], vl8, sWS, hso0 + ((s) & ~3) * 1024, ((s)&3) * 1024 + 512); } \
        else LD4V(hf[s], vl16, sWS, hso0 + ((s) & ~3) * 1024, ((s)&3) * 1024);                                     \
        MARK(h, s);                                                                                                \
    } while (0)
        constexpr int HPF = 10;
#pragma unroll
        for (int s = 0; s < HPF; ++s) G2_LD(s);
#pragma unroll
        for (int s = 0; s < 18; ++s) {
            NEED(h, s);
            if (s == 4 || s == 13) { WAITV_V2(VM(h, s), hs[s == 4 ? 0 : 2], hs[s == 4 ? 1 : 3]); hf[s] = cat(hs[s == 4 ? 0 : 2], hs[s == 4 ? 1 : 3]); }
            else WAITV_V1(VM(h, s), hf[s]);
            if (s + HPF < 18) G2_LD(s + HPF);
            if (s == 11) BARE();
            if (s >= 8 && s < 11) { W3_P1(3 * s - 24); W3_P1(3 * s - 23); W3_P1(3 * s - 22); }
            if (s >= 11 && s < 16) { W3_P0(2 * s - 22); W3_P0(2 * s - 21); }
#pragma unroll
            for (int t = 0; t < 6; ++t) {
                const v4u wf = cat(wl[s & 1][t], wh[s & 1][t]);
                if (s == 0) MFMA_VV0(acc[t], wf, hf[s]); else MFMA_VV(acc[t], wf, hf[s]);
                if (s < 17) G2_RD(s + 1, t);
                SB();
            }
        }
        NOP8();
        asm volatile("" : "+v"(acc[0]), "+v"(acc[1]), "+v"(acc[2]), "+v"(acc[3]), "+v"(acc[4]), "+v"(acc[5]));
        {
            float s1 = 0.f, s2 = 0.f;
            v4u o[3];
#pragma unroll
            for (int t = 0; t < 6; ++t) {
#pragma unroll
                for (int h2 = 0; h2 < 2; ++h2) {
                    const unsigned hv = pk2(acc[t][2 * h2], acc[t][2 * h2 + 1]);
                    const unsigned xv = xres[t][h2];
                    o[t >> 1][(t & 1) * 2 + h2] = pk2(blo(xv) + blo(hv), bhi(xv) + bhi(hv));
                }
                if (t == 1) ST4(o[0], vl16, sWS, WS_X1F + tile * 18432 + ca * 16, 0);
                if (t == 3) ST4(o[1], vl16, sWS, WS_X1F + tile * 18432 + ca * 16, 1024);
                if (t == 5) ST4(o[2], vl16, sWS, WS_X1F + tile * 18432 + cb * 16, 0);
            }
#pragma unroll
            for (int i = 0; i < 12; ++i) {
                const unsigned y = o[i >> 2][i & 3];
                s1 = __builtin_amdgcn_fdot2_f32_bf16(__builtin_bit_cast(bf16x2, y), __builtin_bit_cast(bf16x2, 0x3f803f80u), s1, false);
                s2 = __builtin_amdgcn_fdot2_f32_bf16(__builtin_bit_cast(bf16x2, y), __builtin_bit_cast(bf16x2, y), s2, false);
            }
            v2u st; st.x = __float_as_uint(xsum4(s1)); st.y = __float_as_uint(xsum4(s2));
            ST2(st, g == 0 ? vl8 : OOB, sWS, WS_ST + (tile * 6 + c6) * 128, 0);
            WAITV(0);
            ST2(tagR, lane < 8 ? lane * 6144 : OOB, sWS, fb + F2_OFF + bt2 * 1536 + (c6 * 2 + (w & 1)) * 128, 0);
        }
        BARE();
    } else {
        unsigned junk = 0;
        {
            const int wg = (wv - 48) * 8 + xcd;
#pragma unroll
            for (int i = 0; i < 5; ++i) TOUCH(junk, lane * 64, sWS, wg + 64 * i < 288 ? WS_HF + (wg + 64 * i) * 4096 : WS_CS + (wg + 64 * i - 288) * 4096 + (wg + 64 * i < 294 ? 0 : OOB));
#pragma unroll
            for (int i = 0; i < 3; ++i) TOUCH(junk, lane * 128, sWS, (wg + 64 * i) * 8192);
        }
        for (int i = 0; i < 6; ++i) __builtin_amdgcn_s_sleep(16);
        asm volatile("buffer_load_dwordx2 %0, %1, %2, %3 offen sc1\n\ts_waitcnt vmcnt(0)" : "=&v"(tagT) : "v"(0u), "s"(sWS), "s"(team * FT));
        OPQ(junk);
        tagT.x += 1;
    }
    asm volatile("" : "+s"(w));
    if (j < 18 || j >= 30) {
        {
            asm volatile("s_mov_b32 m0, %0\n\ts_nop 0" ::"s"(lw + LC));
            const int w80 = (j < 18 ? j : j - 12) * 4 + w;
#pragma unroll
            for (int i = 0; i < 7; ++i) WARM(vl16, sW3, (w80 + 80 * i) * 9216 + 576 * 2 * xcd);
            WARM_W3(560 + w80);
            WARM_W3(w80 < 8 ? 640 + w80 : 0);
        }
#pragma unroll
        for (int i = 0; i < 18; ++i) { W3_P0(i); W3_P1(i); }
#pragma unroll
        for (int i = 0; i < 4; ++i) W3_P2(i);
        if (publisher) {
            const int P = j & 1;
            v2u tg;
            asm volatile("buffer_load_dwordx2 %0, %1, %2, 0 offen sc1\n\ts_waitcnt vmcnt(0)\n\ts_barrier" : "=&v"(tg) : "v"((lane & 7) * FT), "s"(sWS) : "memory");
            tg.x += 1;
            unsigned lapw = L0 + (P ? LB : LA) + (g >> 1) * 1024 + (4 * (g & 1) + q) * 128 + ((w ^ ((2 * (g & 1) + (q >> 1)) & 3)) << 5) + (lane & 3) * 8;
            f32x4 cs;
            v2u cl[18], ch[18];
            OPQ(lapw);
#pragma unroll
            for (int s = 0; s < 4; ++s) { cl[s] = trd(lapw + s * 4096); ch[s] = trd(lapw + s * 4096 + 2048); }
#pragma unroll
            for (int s = 0; s < 18; ++s) {
                if (s == 0) MFMA_VV0(cs, cat(cl[s], ch[s]), ones); else MFMA_VV(cs, cat(cl[s], ch[s]), ones);
                if (s + 4 < 18) { cl[s + 4] = trd(lapw + (s + 4) * 4096); ch[s + 4] = trd(lapw + (s + 4) * 4096 + 2048); }
                SB();
            }
            NOP8();
            OPQ(cs);
            ST4X(cs, m == 0 ? g * 16 : OOB, sWS, WS_CS + (xcd * 4 + sub) * 768 + P * 256 + w * 64, 0);
            WAITV(0);
            ST2X(tg, lane < 8 ? lane * FT : OOB, sWS, F5_OFF + (xcd * 4 + sub) * 1024 + P * 512 + w * 128, 0);
        } else {
            WAITV(40);
            asm volatile("s_mov_b32 m0, %0\n\ts_nop 0" ::"s"(L0 + LM + 1024));
            const int wq = (j - (j > 3 ? 2 : 0) - (j > 13 ? 2 : 0)) * 4 + w;
#pragma nounroll
            for (int c = wq; c < 648; c += 48) WARM(vl16, sW4, 576 * 1152 * xcd + c * 1024);
        }
        BARV(0);
    }

    asm volatile("" : "+s"(w));
    const int tile0 = team * 8 + 2 * w;
    v4u X[2][18];
    const unsigned x1_so = WS_X1F + tile0 * 18432;
#define LOAD_X(mt, s) LD4V(X[mt][s], vl16, sWS, x1_so + (mt)*18432 + ((s) >> 2) * 4096, ((s)&3) * 1024)
    constexpr int PF = 5;
    POLL(vf12, sWS, team * FT + F2_OFF + xcd * 6144 + w * 1536, tagT, dead);
#pragma unroll
    for (int s = 0; s < PF; ++s) { LOAD_X(0, s); LOAD_X(1, s); MARK(x, s); }
    LDS_SET(oC, 10);
    AWAIT(aL, 7);
    v2u wl[9], wh[9];
#define RDL(t, o, oc) wl[t] = trd((t) < 8 ? lap[(t)&7] + (o) : lac + (oc))
#define RDH(t, o, oc) wh[t] = trd((t) < 8 ? lap[(t)&7] + (o) + 2048 : lac + (oc) + 512)
#define BUMP(t0, t1, d) _Pragma("unroll") for (int t_ = (t0); t_ < (t1); ++t_) { lap[t_] += (d); OPQ(lap[t_]); }
#pragma unroll
    for (int t = 0; t < 9; ++t) { RDL(t, 0, 0); RDH(t, 0, 0); }
    f32x4 acc[2][9], csv[9];
    SB();
#pragma unroll
    for (int s = 0; s < 18; ++s) {
        NEED(x, s);
        WAITV_V2(VM(x, s), X[0][s], X[1][s]);
        if (s == 5 || s == 10 || s == 14) LDS_SET(oC, s == 5 ? 11 : s == 10 ? 12 : 13);
        if (s == 7) chk = LDS_RD(aL);
        if (s == 8) LDS_WAIT(chk, aL, 8, dead);
        if (s == 12) chk = LDS_RD(aL);
        if (s == 13) LDS_WAIT(chk, aL, 9, dead);
        if (s == 17) chk = LDS_RD(aS);
        if (s == 15) BUMP(0, 8, 65536);
        const int o = ((s + 1) & 15) * 4096, oc = ((s + 1) % 14) * 1024;
#pragma unroll
        for (int t = 0; t < 9; ++t) {
            const v4u wf = cat(wl[t], wh[t]);
            if (s == 0) MFMA_VV0(acc[0][t], wf, X[0][s]); else MFMA_VV(acc[0][t], wf, X[0][s]);
            if (s < 17 && t > 0) RDH(t - 1, o, oc);
            if (s == 0) MFMA_VV0(acc[1][t], wf, X[1][s]); else MFMA_VV(acc[1][t], wf, X[1][s]);
            if (s < 17) RDL(t, o, oc);
            if (t == 8) { if (s == 0) MFMA_VV0(csv[8], wf, ones); else MFMA_VV(csv[8], wf, ones); }
            if (s < 17 && t == 8) RDH(8, o, oc);
            if (s + PF < 18 && t == 1) LOAD_X(0, s + PF);
            if (s + PF < 18 && t == 5) { LOAD_X(1, s + PF); MARK(x, s + PF); }
            SB();
        }
    }
    LDS_WAIT(chk, aS, 1, dead);
    LDS_SET(oC, 14);
    BUMP(0, 8, -65536);
    float nmean[2], c1[2], c2[2], ia[2];
    {
        f32x2 sv[2];
#pragma unroll
        for (int mt = 0; mt < 2; ++mt) sv[mt] = *(LAS f32x2*)(L0 + LM + 1024 + (w * 32 + mt * 16 + m) * 8);
        unsigned csr = L0 + LM + g * 16;
        OPQ(csr);
#pragma unroll
        for (int t = 0; t < 8; ++t) csv[t] = *(LAS f32x4*)(csr + t * 64);
#pragma unroll
        for (int mt = 0; mt < 2; ++mt) {
            const float a = sv[mt].y;
            nmean[mt] = sv[mt].x; c1[mt] = a * GK1; c2[mt] = a * a * a * GK2; ia[mt] = __builtin_amdgcn_rcpf(a);
        }
    }
    v4u own[2][4];
    v2u fl;
    v4u Hf[2][12], Hc[2][2];
    v2u hc[2][2][2];
    const unsigned hh_so = WS_HH + (tile0 * 144 + 18 * xcd + 4 * sub) * 1024;
    const unsigned f3_so = team * FT + F3_OFF + (xcd * 4 + w) * 1536;
    const unsigned h_so = WS_HH + (tile0 * 144 + 18 * xcd) * 1024;
    unsigned hb[3];
#pragma unroll
    for (int i = 0; i < 3; ++i) hb[i] = h_so + ((sub + 1 + i) & 3) * 4096;
#define UBLK(u) ((u) < 4 ? 0 : (u) < 10 ? 1 + (((u)-4) >> 1) : (u) < 16 ? 1 + (((u)-10) >> 1) : 4)
#define UOFF(u) ((u) < 4 ? (u) : (u) < 10 ? ((u)&1) : (u) < 16 ? 2 + ((u)&1) : (u)-16)
#define LOAD_H(mt, u) LD4V(Hf[mt][(u)-4], vl16, sWS, hb[UBLK(u) - 1] + (mt)*147456, UOFF(u) * 1024)
#define LOAD_H2(u) do { LOAD_H(0, u); LOAD_H(1, u); MARK(hh, u); } while (0)
#pragma unroll
    for (int c = 0; c < 8; ++c) {
        const int tp = c >> 1, mt = c & 1;
        if (c == 5) { NEED(sa, 0); WAITV(VM(sa, 0)); ST2(tagT, vone, sWS, f3_so + sub * 128, 0); }
        own[mt][tp] = gelu8(acc[mt][2 * tp], acc[mt][2 * tp + 1], csv[2 * tp], csv[2 * tp + 1], nmean[mt], c1[mt], c2[mt], ia[mt]);
        ST4(own[mt][tp], vl16, sWS, hh_so + mt * 147456, tp * 1024);
        if (c == 3) MARK(sa, 0);
        SB();
    }
    {
        f32x2 v[4] = {{acc[0][8][0], acc[0][8][1]}, {acc[0][8][2], acc[0][8][3]}, {acc[1][8][0], acc[1][8][1]}, {acc[1][8][2], acc[1][8][3]}}, u[4];
        const f32x2 cs[2] = {{csv[8][0], csv[8][1]}, {csv[8][2], csv[8][3]}};
        G8(v[i] = __builtin_elementwise_fma(cs[i & 1], f32x2{nmean[i >> 1], nmean[i >> 1]}, v[i]));
        G8(u[i] = v[i] * v[i]);
        G8(u[i] = __builtin_elementwise_fma(u[i], f32x2{c2[i >> 1], c2[i >> 1]}, f32x2{c1[i >> 1], c1[i >> 1]}));
        G8(u[i] = v[i] * u[i]);
        G8(u[i].x = __builtin_amdgcn_exp2f(u[i].x); u[i].y = __builtin_amdgcn_exp2f(u[i].y));
        G8(u[i] = __builtin_elementwise_fma(u[i], f32x2{ia[i >> 1], ia[i >> 1]}, f32x2{ia[i >> 1], ia[i >> 1]}));
        G8(u[i].x = __builtin_amdgcn_rcpf(u[i].x); u[i].y = __builtin_amdgcn_rcpf(u[i].y));
        G8(u[i] = v[i] * u[i]);
        LD2V(fl, vf4, sWS, f3_so, 0);
        MARK(fl, 0);
#pragma unroll
        for (int mt = 0; mt < 2; ++mt) {
            v2u o; o.x = pk2(u[2 * mt].x, u[2 * mt].y); o.y = pk2(u[2 * mt + 1].x, u[2 * mt + 1].y);
            ST2(o, vl8, sWS, WS_HH + ((tile0 + mt) * 144 + 18 * xcd + 16 + (sub >> 1)) * 1024 + (sub & 1) * 512, 0);
        }
        MARK(sb, 0);
    }
    AWAIT(aL, 10);
#pragma unroll
    for (int t = 0; t < 9; ++t) { RDL(t, 0, 4096); RDH(t, 0, 4096); }
    SB();
    const unsigned pp_so = WS_PP + (((team * 4 + sub) * 8 + 2 * w) * 8 + xcd) * 5120;
    const unsigned f4_so = team * FT + F4_OFF + (sub * 4 + w) * 3072;
#define G4_MFMA(ac, mt, wf, s)                                                               \
    do {                                                                                     \
        if ((s) == 0) MFMA_VV0(ac, wf, own[mt][0]);                                          \
        else if ((s) < 4) MFMA_VV(ac, wf, own[mt][(s) < 4 ? (s) : 0]);                       \
        else if ((s) < 16) MFMA_VV(ac, wf, Hf[mt][(s) >= 4 && (s) < 16 ? (s)-4 : 0]);        \
        else MFMA_VV(ac, wf, Hc[mt][(s) >= 16 ? (s)-16 : 0]);                                \
    } while (0)
#define PART_STAGE(kk, P)                                                                    \
    do {                                                                                     \
        const int id_ = (kk) >> 1, mt_ = id_ >> 2, t_ = id_ & 3, e_ = ((kk)&1) * 2;          \
        po[(t_ & 1) * 2 + ((kk)&1)] = pk2(acc[mt_][4 * (P) + t_][e_], acc[mt_][4 * (P) + t_][e_ + 1]); \
        if (((kk)&3) == 3) ST4(po, vl16, sWS, pp_so + mt_ * 40960, (2 * (P) + (t_ >> 1)) * 1024); \
    } while (0)
    v4u po;
#pragma unroll
    for (int s = 0; s < 10; ++s) {
        if (s == 1) { NEED(fl, 0); POLL_CHECK(VM(fl, 0), fl, vf4, sWS, f3_so, tagT, dead); }
        if (s == 2) { NEED(sb, 0); WAITV(VM(sb, 0)); ST2(tagT, vone, sWS, f3_so + 512 + sub * 128, 0); }
        if (s == 4) { LD2V(fl, vf4, sWS, f3_so, 512); MARK(fl, 1); }
        if (s >= 4) { NEED(hh, s); WAITV_V2(VM(hh, s), Hf[0][s >= 4 ? s - 4 : 0], Hf[1][s >= 4 ? s - 4 : 0]); }
        if (s == 5) LDS_SET(oC, 15);
        if (s == 8) chk = LDS_RD(aL);
        if (s == 9) LDS_WAIT(chk, aL, 11, dead);
        if (s == 6) { NEED(fl, 1); POLL_CHECK(VM(fl, 1), fl, vf4, sWS, f3_so + 512, tagT, dead); }
        const int o = (s + 1) * 4096, oc = (s + 5) * 1024;
#pragma unroll
        for (int t = 0; t < 9; ++t) {
            const v4u wf = cat(wl[t], wh[t]);
            G4_MFMA(acc[0][t], 0, wf, s);
            if (t > 0 && (s < 9 || t <= 4)) RDH(t - 1, o, oc);
            G4_MFMA(acc[1][t], 1, wf, s);
            if (s < 9 || t < 4) RDL(t, o, oc);
            if (s < 9 && t == 8) RDH(8, o, oc);
            if (s == 1 && (t & 1) && t < 7) LOAD_H2(4 + (t >> 1));
            if (s == 2 && (t & 1) && t < 7) LOAD_H2(7 + (t >> 1));
            if (s >= 6 && s < 9 && (t == 1 || t == 5)) LOAD_H2(10 + 2 * (s - 6) + (t >> 2));
            if (s == 9 && t < 8) { LD2V(hc[(t >> 1) & 1][t >> 2][t & 1], vl8, sWS, h_so + 16384 + ((t >> 1) & 1) * 147456, (t >> 2) * 1024 + (t & 1) * 512); if ((t & 3) == 3) MARK(hh, 16 + (t >> 2)); }
            SB();
        }
    }
    v2u xl[3][4], xh[3][4];
#pragma unroll
    for (int t = 0; t < 4; ++t) { xl[0][t] = wl[t]; xh[0][t] = wh[t]; xl[1][t] = trd(lap[t] + 11 * 4096); xh[1][t] = trd(lap[t] + 11 * 4096 + 2048); }
#pragma unroll
    for (int P = 0; P < 2; ++P) {
#pragma unroll
        for (int s = 10; s < 18; ++s) {
            if (P == 0) {
                NEED(hh, s);
                if (s < 16) WAITV_V2(VM(hh, s), Hf[0][s < 16 ? s - 4 : 0], Hf[1][s < 16 ? s - 4 : 0]);
                else {
                    const int u = s >= 16 ? s - 16 : 0;
                    asm volatile("s_waitcnt vmcnt(%4)" : "+v"(hc[0][u][0]), "+v"(hc[0][u][1]), "+v"(hc[1][u][0]), "+v"(hc[1][u][1]) : "i"(VM(hh, s)));
                    Hc[0][u] = cat(hc[0][u][0], hc[0][u][1]); Hc[1][u] = cat(hc[1][u][0], hc[1][u][1]);
                }
            }
            if (P == 1 && s == 17) { NEED(p0, 0); WAITV(VM(p0, 0)); ST2(tagT, vone, sWS, f4_so + xcd * 128, 0); }
            if (s == 14) BUMP(4 * P, 4 * P + 4, 65536);
            if (P == 1 && s == 15) chk = LDS_RD(aL);
            if (P == 1 && s == 16) LDS_WAIT(chk, aL, 12, dead);
            const int li = 8 * P + s - 10, np = (li + 2) >> 3;
            const unsigned nb = ((10 + ((li + 2) & 7)) & 15) * 4096;
#pragma unroll
            for (int t = 0; t < 4; ++t) {
                const v4u wf = cat(xl[li % 3][t], xh[li % 3][t]);
                G4_MFMA(acc[0][4 * P + t], 0, wf, s);
                if (li < 14) xl[(li + 2) % 3][t] = trd(lap[4 * (np & 1) + t] + nb);
                else { wl[(li - 14) * 4 + t] = trd(lac + ((li - 14) * 4 + t) * 1024); }
                if (P == 1 && (s == 11 || s == 12)) PART_STAGE((s - 11) * 8 + 2 * t, 0);
                G4_MFMA(acc[1][4 * P + t], 1, wf, s);
                if (li < 14) xh[(li + 2) % 3][t] = trd(lap[4 * (np & 1) + t] + nb + 2048);
                else { wh[(li - 14) * 4 + t] = trd(lac + ((li - 14) * 4 + t) * 1024 + 512); }
                if (P == 1 && (s == 11 || s == 12)) PART_STAGE((s - 11) * 8 + 2 * t + 1, 0);
                if (P == 1 && s == 12 && t == 3) MARK(p0, 0);
                SB();
            }
        }
    }
    asm volatile("" : "+s"(w));
    const unsigned fr_so = team * FT + F4_OFF + (sub * 4 + (xcd >> 1)) * 3072 + (w >> 1) * 1024;
    const unsigned pr_so = WS_PP + (((team * 4 + sub) * 8 + xcd) * 8) * 5120;
    const unsigned oso = (team * 8 + xcd) * 16 * 1152;
    const unsigned vo = m * 1152 + g * 8, vfr = (lane < 8 ? lane : 0) * 128;
    v4u xr;
    v2u xc;
#pragma unroll
    for (int s = 10; s < 18; ++s) {
        const v4u wf = cat(wl[s - 10], wh[s - 10]);
        G4_MFMA(acc[0][8], 0, wf, s);
        if (s > 10) PART_STAGE(2 * (s - 11), 1);
        G4_MFMA(acc[1][8], 1, wf, s);
        if (s > 10) PART_STAGE(2 * (s - 11) + 1, 1);
        SB();
    }
    PART_STAGE(14, 1);
    PART_STAGE(15, 1);
    MARK(p1, 0);
    asm volatile("s_nop 1" : "+v"(acc[0][8]), "+v"(acc[1][8]));
#pragma unroll
    for (int mt = 0; mt < 2; ++mt) {
        v4u o; o.x = pk2(acc[mt][8][0], acc[mt][8][1]); o.y = tagT.x; o.z = pk2(acc[mt][8][2], acc[mt][8][3]); o.w = tagT.x;
        ST4(o, vl16, sWS, pp_so + mt * 40960 + 4096, 0);
    }
    LD2V(fl, vfr, sWS, fr_so, 0);
    MARK(fr, 0);
    LD4V(xr, vl16, sWS, WS_X1F + ((team * 8 + xcd) * 18 + 4 * sub + w) * 1024, 0);
    LD2V(xc, vl16, sWS, WS_X1F + ((team * 8 + xcd) * 18 + 16 + (sub >> 1)) * 1024 + (sub & 1) * 8, 0);
    NEED(p1, 0);
    WAITV(VM(p1, 0));
    ST2(tagT, vone, sWS, f4_so + 1024 + xcd * 128, 0);
    {
        NEED(fr, 0);
        POLL_CHECK(VM(fr, 0), fl, vfr, sWS, fr_so, tagT, dead);
        v4u p[8], c[8];
        const unsigned vc = w == 1 ? vl16 : OOB;
#pragma unroll
        for (int i = 0; i < 8; ++i) LD4V(p[i], vl16, sWS, pr_so + i * 5120 + w * 1024, 0);
#pragma unroll
        for (int i = 0; i < 8; ++i) LD4V(c[i], vc, sWS, pr_so + i * 5120 + 4096, 0);
        asm volatile("s_waitcnt vmcnt(0)" : "+v"(p[0]), "+v"(p[1]), "+v"(p[2]), "+v"(p[3]), "+v"(p[4]), "+v"(p[5]), "+v"(p[6]), "+v"(p[7]), "+v"(xr), "+v"(xc));
        asm volatile("" : "+v"(c[0]), "+v"(c[1]), "+v"(c[2]), "+v"(c[3]), "+v"(c[4]), "+v"(c[5]), "+v"(c[6]), "+v"(c[7]));
        v4u o;
#pragma unroll
        for (int e = 0; e < 4; ++e) {
            f32x2 a = {0.f, 0.f};
#pragma unroll
            for (int i = 0; i < 8; ++i) a += f32x2{blo(p[i][e]), bhi(p[i][e])};
            const unsigned hv = pk2(a.x, a.y);
            o[e] = pk2(blo(xr[e]) + blo(hv), bhi(xr[e]) + bhi(hv));
        }
        v2u o0 = {o.x, o.y}, o1 = {o.z, o.w};
        ST2P(o0, vo, sO, oso + (128 * sub + 32 * w) * 2, 0);
        ST2P(o1, vo, sO, oso + (128 * sub + 32 * w) * 2, 32);
        if (w == 1) {
            for (;;) {
                unsigned bad = 0;
#pragma unroll
                for (int i = 0; i < 8; ++i) bad |= (c[i].y ^ tagT.x) | (c[i].w ^ tagT.x);
                if (__all(bad == 0) || dead == 0) break;
                --dead;
#pragma unroll
                for (int i = 0; i < 8; ++i) LD4V(c[i], vl16, sWS, pr_so + i * 5120 + 4096, 0);
                asm volatile("s_waitcnt vmcnt(0)" : "+v"(c[0]), "+v"(c[1]), "+v"(c[2]), "+v"(c[3]), "+v"(c[4]), "+v"(c[5]), "+v"(c[6]), "+v"(c[7]));
            }
            v2u oc;
#pragma unroll
            for (int e = 0; e < 2; ++e) {
                f32x2 a = {0.f, 0.f};
#pragma unroll
                for (int i = 0; i < 8; ++i) a += f32x2{blo(c[i][2 * e]), bhi(c[i][2 * e])};
                const unsigned hv = pk2(a.x, a.y);
                oc[e] = pk2(blo(xc[e]) + blo(hv), bhi(xc[e]) + bhi(hv));
            }
            ST2P(oc, vo, sO, oso + (512 + 16 * sub) * 2, 0);
        }
        if (xcd == 0 && sub == 0 && w == 0) ST2(tagT, vone, sWS, team * FT, 0);
    }
}

extern "C" void kernel_launch(void* const* d_in, const int* in_sizes, int n_in, void* d_out, int out_size, void* d_ws, size_t ws_size,
                              hipStream_t stream) {
    standalone__mixer_s_mlp__sampling_kernel<<<256, 512, 0, stream>>>((const u16*)d_in[0], (const u16*)d_in[1], (const u16*)d_in[2], (const u16*)d_in[3],
                                          (const u16*)d_in[4], (u16*)d_out, (unsigned char*)d_ws + WS_OFFSET(ws_size));
}
```
